```python
import torch

def _softmax(x, axis=-1):
    return torch.softmax(x, dim=axis)

def _gelu(x):
    return torch.nn.functional.gelu(x, approximate='tanh')

def _silu(x):
    return torch.nn.functional.silu(x)

def _sigmoid(x):
    return torch.sigmoid(x)

def _layernorm(x, axis=-1, eps=1e-05):
    m = x.mean(dim=axis, keepdim=True)
    v = x.var(dim=axis, keepdim=True, unbiased=False)
    return (x - m) / torch.sqrt(v + eps)

def _scatter_add(tgt, idx, src, axis=0):
    return tgt.clone().index_add_(axis, idx, src)

def _segment_sum(src, seg, n):
    out = torch.zeros((n,) + src.shape[1:], dtype=src.dtype)
    return out.index_add_(0, seg, src)

def _pad(x, pw):
    flat = tuple((v for pair in reversed(pw) for v in pair))
    return torch.nn.functional.pad(x, flat)
kernel_name = 'concat_heads__memory_aug_recurrent_mem_tr__grouped_kv_mqa__prompt'

def setup_inputs():

    _w = lambda a: (lambda t: t.to(torch.bfloat16) if t.is_floating_point() else t)(torch.as_tensor(a))

    def _A_setup():
        B, S, Dm = (1, 2048, 576)
        H, Dh = (32, 128)
        M, seg = (8, 32)
        seg = min(seg, S)
        S = S // seg * seg
        return {'x': _w((lambda *s: torch.randn(*s, dtype=torch.bfloat16) * 0.05)(B, S, Dm)), 'mem0': _w((lambda *s: torch.randn(*s, dtype=torch.bfloat16) * 0.8)(B, M, Dm)), 'Wq': _w((lambda *s: torch.randn(*s, dtype=torch.bfloat16) * 1.6)(Dm, H, Dh) * Dm ** (-0.5)), 'Wk': _w((lambda *s: torch.randn(*s, dtype=torch.bfloat16) * 1.6)(Dm, H, Dh) * Dm ** (-0.5)), 'Wv': _w((lambda *s: torch.randn(*s, dtype=torch.bfloat16) * 0.8)(Dm, H, Dh) * Dm ** (-0.5)), 'Wo': _w((lambda *s: torch.randn(*s, dtype=torch.bfloat16) * 0.8)(H, Dh, Dm) * (H * Dh) ** (-0.5)), 'seg_len': seg}
    _A_in = _A_setup()

    def _B_setup():
        B, S, D, H = (1, 2048, 576, 32)
        Dh = D // H
        return {'x': _w((lambda *s: torch.randn(*s, dtype=torch.bfloat16) * 0.05)(B, S, D)), 'wq': _w((lambda *s: torch.randn(*s, dtype=torch.bfloat16) * 1.6)(D, H * Dh)), 'wk': _w((lambda *s: torch.randn(*s, dtype=torch.bfloat16) * 1.6)(D, Dh)), 'wv': _w((lambda *s: torch.randn(*s, dtype=torch.bfloat16) * 0.05)(D, Dh)), 'wo': _w((lambda *s: torch.randn(*s, dtype=torch.bfloat16) * 0.05)(H * Dh, D))}
    _B_in = _B_setup()
    return {'A_x': _A_in['x'], 'A_mem0': _A_in['mem0'], 'A_Wq': _A_in['Wq'], 'A_Wk': _A_in['Wk'], 'A_Wv': _A_in['Wv'], 'A_Wo': _A_in['Wo'], 'A_seg_len': _A_in['seg_len'], 'B_x': _B_in['x'], 'B_wq': _B_in['wq'], 'B_wk': _B_in['wk'], 'B_wv': _B_in['wv'], 'B_wo': _B_in['wo']}

def reference(A_x, A_mem0, A_Wq, A_Wk, A_Wv, A_Wo, A_seg_len, B_x, B_wq, B_wk, B_wv, B_wo):

    def _A_ref(x, mem0, Wq, Wk, Wv, Wo, seg_len):
        B, S, Dm = x.shape
        H, Dh = (Wq.shape[1], Wq.shape[2])
        M = mem0.shape[1]
        scale = Dh ** (-0.5)
        n_seg = S // seg_len
        L = M + seg_len
        idx = torch.arange(L)
        mask = (idx[:, None] < M) | (idx[:, None] >= idx[None, :])
        mem = mem0
        outs = []
        for s in range(n_seg):
            xs = x[:, s * seg_len:(s + 1) * seg_len, :].clone()
            h = torch.concatenate([mem, xs], axis=1)
            q = torch.einsum('btd,dhe->bhte', h, Wq)
            k = torch.einsum('btd,dhe->bhte', h, Wk)
            v = torch.einsum('btd,dhe->bhte', h, Wv)
            scores = torch.einsum('bhqe,bhke->bhqk', q, k) * scale
            scores = torch.where(mask[None, None], scores, -1000000000.0)
            p = _softmax(scores, axis=-1)
            o = torch.einsum('bhqk,bhke->bhqe', p, v)
            o = torch.einsum('bhte,hed->btd', o, Wo)
            mem = o[:, :M, :].clone()
            outs.append(o[:, M:, :])
        return torch.concatenate(outs, axis=1)

    def _B_ref(x, wq, wk, wv, wo):
        B, S, D = x.shape
        H = 32
        Dh = D // H
        q = (lambda x, *d: x.permute(*d))((x @ wq).reshape(B, S, H, Dh), 0, 2, 1, 3)
        k = x @ wk
        v = x @ wv
        scores = torch.einsum('bhqd,bkd->bhqk', q, k) * Dh ** (-0.5)
        p = _softmax(scores, axis=-1)
        o = torch.einsum('bhqk,bkd->bhqd', p, v)
        return (lambda x, *d: x.permute(*d))(o, 0, 2, 1, 3).reshape(B, S, H * Dh) @ wo
    a = _A_ref(A_x, A_mem0, A_Wq, A_Wk, A_Wv, A_Wo, A_seg_len)
    b = _B_ref(B_x, B_wq, B_wk, B_wv, B_wo)
    return torch.concatenate([a, b], axis=-1)
```

```cpp
#include <hip/hip_runtime.h>
#include <hip/hip_bf16.h>
typedef unsigned short u16;
__device__ __forceinline__ float b2f(u16 x) { return __uint_as_float((unsigned)x << 16); }
__device__ __forceinline__ u16 f2b(float f) { __bf16 b = (__bf16)f; return __builtin_bit_cast(u16, b); }
__global__ void gemm(const u16* A, int lda, const u16* W, int ldw, u16* C, int ldc, int M, int N, int K) {
    int n = blockIdx.x * 64 + (threadIdx.x & 63), m0 = (blockIdx.y * 4 + (threadIdx.x >> 6)) * 8;
    if (n >= N || m0 >= M) return;
    float acc[8] = {0, 0, 0, 0, 0, 0, 0, 0};
    for (int k = 0; k < K; ++k) { float w = b2f(W[(size_t)k * ldw + n]);
#pragma unroll
        for (int i = 0; i < 8; ++i) acc[i] += b2f(A[(size_t)(m0 + i) * lda + k]) * w; }
#pragma unroll
    for (int i = 0; i < 8; ++i) C[(size_t)(m0 + i) * ldc + n] = f2b(acc[i]);
}
__global__ void attnA(const u16* q, const u16* k, const u16* v, u16* o) {
    __shared__ float s[40][41];
    int h = blockIdx.x, t = threadIdx.x;
    for (int i = t; i < 1600; i += 256) { int r = i / 40, c = i % 40; float a = 0; for (int e = 0; e < 128; ++e) a += b2f(q[r * 4096 + h * 128 + e]) * b2f(k[c * 4096 + h * 128 + e]);
        s[r][c] = (r < 8 || r >= c) ? a * 0.08838834764831845f : -1e9f; }
    __syncthreads();
    if (t < 40) { float m = -1e30f; for (int c = 0; c < 40; ++c) m = fmaxf(m, s[t][c]); float l = 0; for (int c = 0; c < 40; ++c) { s[t][c] = __expf(s[t][c] - m); l += s[t][c]; } for (int c = 0; c < 40; ++c) s[t][c] /= l; }
    __syncthreads();
    for (int i = t; i < 40 * 128; i += 256) { int r = i / 128, e = i % 128; float a = 0; for (int c = 0; c < 40; ++c) a += s[r][c] * b2f(v[c * 4096 + h * 128 + e]); o[r * 4096 + h * 128 + e] = f2b(a); }
}
__global__ void attnB(const u16* q, const u16* k, const u16* v, u16* o) {
    int i = blockIdx.x * 256 + threadIdx.x; int r = i / 32, h = i % 32;
    float qq[18], acc[18]; for (int d = 0; d < 18; ++d) { qq[d] = b2f(q[r * 576 + h * 18 + d]) * 0.23570226039551584f; acc[d] = 0; }
    float m = -1e30f, l = 0;
    for (int t = 0; t < 2048; ++t) { float s = 0; for (int d = 0; d < 18; ++d) s += qq[d] * b2f(k[t * 18 + d]);
        float mn = fmaxf(m, s), c = __expf(m - mn), p = __expf(s - mn); l = l * c + p; for (int d = 0; d < 18; ++d) acc[d] = acc[d] * c + p * b2f(v[t * 18 + d]); m = mn; }
    for (int d = 0; d < 18; ++d) o[r * 576 + h * 18 + d] = f2b(acc[d] / l);
}
__global__ void cp(const u16* s, int lds_, u16* d, int ldd, int rows) { int i = blockIdx.x * 256 + threadIdx.x; if (i < rows * 576) d[(i / 576) * ldd + i % 576] = s[(i / 576) * lds_ + i % 576]; }
extern "C" void kernel_launch(void* const* d_in, const int* in_sizes, int n_in, void* d_out, int out_size, void* d_ws, size_t ws_size, hipStream_t st) {
    const u16 *x = (const u16*)d_in[0], *mem0 = (const u16*)d_in[1], *Wq = (const u16*)d_in[2], *Wk = (const u16*)d_in[3], *Wv = (const u16*)d_in[4], *Wo = (const u16*)d_in[5];
    const u16 *xb = (const u16*)d_in[7], *wq = (const u16*)d_in[8], *wk = (const u16*)d_in[9], *wv = (const u16*)d_in[10], *wo = (const u16*)d_in[11];
    u16* out = (u16*)d_out; u16* ws = (u16*)d_ws;
    u16 *hb = ws, *q = ws + 65536, *k = q + 40 * 4096, *v = k + 40 * 4096, *o = v + 40 * 4096, *res = o + 40 * 4096, *qb = res + 65536, *kb = qb + 2048 * 576, *vb = kb + 2048 * 32, *ob = vb + 2048 * 32;
    cp<<<18, 256, 0, st>>>(mem0, 576, hb, 576, 8);
    for (int s = 0; s < 64; ++s) {
        cp<<<72, 256, 0, st>>>(x + s * 32 * 576, 576, hb + 8 * 576, 576, 32);
        gemm<<<dim3(64, 2), 256, 0, st>>>(hb, 576, Wq, 4096, q, 4096, 40, 4096, 576);
        gemm<<<dim3(64, 2), 256, 0, st>>>(hb, 576, Wk, 4096, k, 4096, 40, 4096, 576);
        gemm<<<dim3(64, 2), 256, 0, st>>>(hb, 576, Wv, 4096, v, 4096, 40, 4096, 576);
        attnA<<<32, 256, 0, st>>>(q, k, v, o);
        gemm<<<dim3(9, 2), 256, 0, st>>>(o, 4096, Wo, 576, res, 576, 40, 576, 4096);
        cp<<<18, 256, 0, st>>>(res, 576, hb, 576, 8);
        cp<<<72, 256, 0, st>>>(res + 8 * 576, 576, out + s * 32 * 1152, 1152, 32);
    }
    gemm<<<dim3(9, 64), 256, 0, st>>>(xb, 576, wq, 576, qb, 576, 2048, 576, 576);
    gemm<<<dim3(1, 64), 256, 0, st>>>(xb, 576, wk, 18, kb, 18, 2048, 18, 576);
    gemm<<<dim3(1, 64), 256, 0, st>>>(xb, 576, wv, 18, vb, 18, 2048, 18, 576);
    attnB<<<256, 256, 0, st>>>(qb, kb, vb, ob);
    gemm<<<dim3(9, 64), 256, 0, st>>>(ob, 576, wo, 576, out + 576, 1152, 2048, 576, 576);
}
```

```cpp
#include <hip/hip_runtime.h>
#include <stdint.h>
typedef unsigned short u16;
typedef unsigned u32;
typedef u32 v2u __attribute__((ext_vector_type(2)));
typedef u32 v4u __attribute__((ext_vector_type(4)));
typedef u32 v8u __attribute__((ext_vector_type(8)));
typedef float f2 __attribute__((ext_vector_type(2)));
typedef float f4 __attribute__((ext_vector_type(4)));
typedef __bf16 b2 __attribute__((ext_vector_type(2)));
typedef __bf16 b4 __attribute__((ext_vector_type(4)));
typedef __bf16 b8 __attribute__((ext_vector_type(8)));
typedef __bf16 b16 __attribute__((ext_vector_type(16)));
typedef short s4 __attribute__((ext_vector_type(4)));
#define LAS __attribute__((address_space(3)))
#define DI __device__ __forceinline__
typedef __amdgpu_buffer_rsrc_t rsrc_t;
#define BC(T, x) __builtin_bit_cast(T, x)
constexpr int SC1 = 16;
DI rsrc_t mkrs(const void* p, u32 bytes) { return __builtin_amdgcn_make_buffer_rsrc((void*)p, 0, bytes, 0x00020000); }
DI v4u bld4(rsrc_t r, u32 vo, u32 so, int aux) { return aux ? __builtin_amdgcn_raw_buffer_load_b128(r, vo, so, SC1) : __builtin_amdgcn_raw_buffer_load_b128(r, vo, so, 0); }
DI u32 bld1(rsrc_t r, u32 vo, u32 so) { return __builtin_amdgcn_raw_buffer_load_b32(r, vo, so, SC1); }
DI void bst4(v4u v, rsrc_t r, u32 vo, u32 so) { __builtin_amdgcn_raw_buffer_store_b128(v, r, vo, so, SC1); }
DI void bst2(v2u v, rsrc_t r, u32 vo, u32 so) { __builtin_amdgcn_raw_buffer_store_b64(v, r, vo, so, SC1); }
DI void bst1(u32 v, rsrc_t r, u32 vo, u32 so) { __builtin_amdgcn_raw_buffer_store_b32(v, r, vo, so, SC1); }
DI u32 pk(float a, float b) { f2 t = {a, b}; return BC(u32, __builtin_convertvector(t, b2)); }
DI v2u trrd(u32 a) { return BC(v2u, __builtin_amdgcn_ds_read_tr16_b64_v4i16((LAS s4*)a)); }
DI v4u lrd4(u32 a) { return *(LAS v4u*)a; }
DI v2u lrd2(u32 a) { return *(LAS v2u*)a; }
DI void lwr4(u32 a, v4u v) { *(LAS v4u*)a = v; }
DI void lwr2(u32 a, v2u v) { *(LAS v2u*)a = v; }
DI f4 smf(v4u a, v8u b, f4 c, u32 idx) { return __builtin_amdgcn_smfmac_f32_16x16x64_bf16(BC(b8, a), BC(b16, b), c, (int)idx, 0, 0); }
DI f4 mf32(v4u a, v4u b, f4 c) { return __builtin_amdgcn_mfma_f32_16x16x32_bf16(BC(b8, a), BC(b8, b), c, 0, 0, 0); }
DI f4 mf16(v2u a, v2u b, f4 c) { return __builtin_amdgcn_mfma_f32_16x16x16bf16_1k(BC(s4, a), BC(s4, b), c, 0, 0, 0); }
DI void swap32(float& a, float& b) { auto r = __builtin_amdgcn_permlane32_swap(BC(u32, a), BC(u32, b), false, false); a = BC(float, (u32)r[0]); b = BC(float, (u32)r[1]); }
DI void swap16(u32& a, u32& b) { auto r = __builtin_amdgcn_permlane16_swap(a, b, false, false); a = r[0]; b = r[1]; }
DI u32 ror8(u32 x) { return (u32)__builtin_amdgcn_update_dpp(0, (int)x, 0x128, 0xf, 0xf, true); }
DI float ror8f(float x) { return BC(float, ror8(BC(u32, x))); }
DI u32 mx4(v4u v) { return max(max(v.x, v.y), max(v.z, v.w)); }
#define BAR() asm volatile("s_waitcnt lgkmcnt(0)\n\ts_barrier" ::: "memory")
#define CLOB() asm volatile("" ::: "memory")
#define SB() __builtin_amdgcn_sched_barrier(0)
DI void dly(int n) { if (n > 0) asm volatile("1:\n\ts_sub_u32 %0, %0, 1\n\ts_cmp_lg_u32 %0, 0\n\ts_cbranch_scc1 1b" : "+s"(n) :: "scc", "memory"); }
DI void dma16(rsrc_t r, u32 vo, u32 so, u32 lds) {
    u32 keep;
    asm volatile("s_mov_b32 %0, m0\n\ts_mov_b32 m0, %4\n\ts_nop 0\n\tbuffer_load_dwordx4 %1, %2, %3 offen lds\n\ts_mov_b32 m0, %0" : "=&s"(keep) : "v"(vo), "s"(r), "s"(so), "s"(lds) : "memory");
}
template <int N> DI void waitvm() { asm volatile("s_waitcnt vmcnt(%0)" :: "i"(N) : "memory"); }

constexpr u32 WS_MEMR = 72u * 16384u;
constexpr u32 WS_FLAG = 92u * 16384u;
constexpr u32 F_CSEL = WS_FLAG;
constexpr u32 F_ERR = WS_FLAG + 256;
constexpr u32 F_PROG = WS_FLAG + 1024;
constexpr u32 F_BK = WS_FLAG + 9216;
constexpr u32 F_OX = WS_FLAG + 32768 + 8 * 16384;
constexpr u32 F_TILE = WS_FLAG + 16384;
constexpr u32 F_TQ = WS_FLAG + 10240;
constexpr u32 WS_INIT_BYTES = 103u * 16384u;
constexpr u32 WS_Q = 2u << 20;
constexpr u32 WS_K = WS_Q + (16u << 20);
constexpr u32 WS_VT = WS_K + (16u << 20);
constexpr u32 WS_KM = WS_VT + (16u << 20);
constexpr u32 WS_VM = WS_KM + (4u << 20);
constexpr u32 WS_OX = WS_VM + (8u << 20);
constexpr u32 WS_DBG = WS_OX + (16u << 20);
constexpr u32 WS_BKV = WS_DBG + (7u << 20);
constexpr u32 WS_END = WS_DBG + (8u << 20);
constexpr u32 SPIN_CAP = 1u << 20;

struct Params {
    const u16 *x, *mem0, *Wq, *Wk, *Wv, *Wo, *xb, *wq, *wk, *wv, *wo;
    u16* out; char* ws;
    int d1, d2, d3, d4, d5, pad;
    __device__ __forceinline__ char* wsa() const { return ws + ((0 - (uintptr_t)ws) & 0x1FFFFF); }
};

constexpr u32 L_WV = 0, L_MEM = 147456, L_SX = L_MEM + 9216, L_OX = L_SX + 5120;

typedef u32 v16u __attribute__((ext_vector_type(16)));
DI void x_role_asm(const Params& p, const int xi, const int rep) {
    const u32 lane = threadIdx.x & 63;
    const u32 w = __builtin_amdgcn_readfirstlane(threadIdx.x >> 6);
    const u32 h = xi >> 1, cc = xi & 1;
    const u32 g = lane >> 4, n = lane & 15, q4 = n >> 2, p4 = lane & 3, u = (lane >> 3) & 1, r = lane & 7;
    for (u32 i = threadIdx.x; i < 1152; i += 256) {
        const u32 U = i >> 4, bb = (i >> 3) & 1, gp = (i >> 2) & 1, pp = i & 3, kb = U >> 3;
        const u32 c0 = kb < 8 ? 288 * (kb >> 2) + 64 * (kb & 3) + 32 * gp + 2 * (U & 7) + bb : 288 * ((U >> 2) & 1) + 256 + 4 * (U & 3) + 2 * bb + gp;
        const u16* m = p.mem0 + 2 * pp * 576 + c0;
        lwr2(L_MEM + i * 8, (v2u){(u32)m[0] | ((u32)m[16] << 16), (u32)m[576] | ((u32)m[592] << 16)});
    }
    const u32 one = 0x3F80u;
    const u32 dd = (r >> 1) == g ? one | (one << 16) : 0, s0 = (r & 1) ? 0 : dd, s1 = (r & 1) ? dd : 0;
    const u32 gr = (g * 8 + r) * 16;
    const v8u c0 = {(n & 1) ? 0xEEEEu : 0x4444u, lane * 16, u ? 0 : s0, u ? 0 : s1, u ? s0 : 0, u ? s1 : 0, 0x3F803F80u, 0x3F803F80u};
    const u32 um = 32 * cc + 8 * w + (n >> 1), ue = 64 + 4 * cc + (n >> 2);
    const v16u c1 = {0x3F803F80u, 0x3F803F80u, one, one,
                     L_MEM + lane * 8, L_OX + (n & 1) * 512 + (g * 8 + (n >> 1)) * 16, 0u,
                     w * 4096 + lane * 16, w * 4096 + lane * 16 + 65536, w * 4096 + lane * 16 + 131072,
                     L_SX + w * 1280 + u * 512 + gr, L_SX + u * 512 + gr, L_SX + 1024 + ((g >> 1) * 8 + r) * 16 + (2 * (g & 1) + u) * 4, L_OX + w * 512 + gr,
                     (um >> 1) * 16384 + (um & 1) * 4096 + h * 128 + ((n & 1) * 4 + g) * 16,
                     (ue >> 1) * 16384 + (ue & 1) * 4096 + h * 128 + (((n >> 1) & 1) * 4 + g) * 16 + (2 * (n & 1) + (w & 1)) * 4};
    v8u c2;
    c2[0] = (4 * w + g) * 8192 + h * 256 + ((n ^ (w & 2)) << 4);
    c2[1] = 65536 + (32 * (q4 >> 1) + 16 * (g & 1) + 4 * (g >> 1) + (q4 & 1)) * 576 + p4 * 8 + w * 128;
    c2[2] = (288 * (w >> 1) + 256 + 8 * (w & 1) + 4 * (g >> 1) + 2 * (g & 1)) * 8192 + h * 256 + ((n ^ (w & 2)) << 4);
    c2[3] = 0; c2[6] = 0; c2[7] = 0;
#pragma unroll
    for (int a = 0; a < 2; ++a) c2[4 + a] = (32 * (q4 & 1) + 16 * (q4 >> 1) + 8 * (g & 1) + 2 * (g >> 1)) * 256 + (((4 * w + 2 * a + (p4 >> 1)) ^ ((4 * (q4 & 1) + 2 * (q4 >> 1) + (g & 1)) << 1)) << 4) + (p4 & 1) * 8;
    const int unit = w == 2 ? xi : (w == 3 && xi < 8) ? 64 + xi : -1;
    const u32 uu = unit < 0 ? 0 : unit, rs = uu < 64 ? 16384 : 1024;
    const u32 oy = one << (16 * (r & 1));
    const v8u c3 = {(r >> 1) == 0 ? oy : 0, (r >> 1) == 1 ? oy : 0, (r >> 1) == 2 ? oy : 0, (r >> 1) == 3 ? oy : 0, (u + (g & 2)) * rs + (r >> 2) * 64 + (g & 1) * 32 + (r & 3) * 8, ((lane >> 3) & 3) * rs + r * 16, 0, 0};
    const uint64_t a0 = (uint64_t)p.wsa(), a1 = (uint64_t)p.Wq, a2 = (uint64_t)p.Wk, a3 = (uint64_t)p.Wv, a4 = (uint64_t)p.Wo;
    v16u sa = {(u32)a0, (u32)(a0 >> 32), (u32)a1, (u32)(a1 >> 32), (u32)a2, (u32)(a2 >> 32), (u32)a3, (u32)(a3 >> 32), (u32)a4, (u32)(a4 >> 32),
               w, (u32)unit, (u32)p.d1, (u32)p.d2, (u32)p.d3, (h * 128 + w * 32) * 1152 + cc * 576};
    v16u sb = {WS_K + h * 8192 + w * 2048, F_TILE + h * 32, WS_KM + h * 2048 - L_OX, WS_VM + h * 4096 + w * 1024, F_PROG + xi * 128,
               (uu >> 1) * 16384 + (uu & 1) * 4096, ((uu >> 3) < 8 ? (uu >> 3) * 1024 : 65536) + (uu & 7) * 128, (16 + (w & 1) - 4 * w) * 16384, cc, w * 1024, (u32)p.d4, (u32)p.d5, (u32)rep, 0, 0, 0};
#pragma unroll
    for (int i = 0; i < 16; ++i) { sa[i] = __builtin_amdgcn_readfirstlane(sa[i]); sb[i] = __builtin_amdgcn_readfirstlane(sb[i]); }
    const u32 so = __builtin_amdgcn_readfirstlane(WS_DBG + (1 << 20) + (xi * 4 + w) * 128);
    asm volatile(
    "s_nop 4\n"
    "s_mov_b32 s8, s16\n"
    "s_mov_b32 s9, s17\n"
    "s_mov_b32 s10, 0x6000000\n"
    "s_mov_b32 s11, 0x20000\n"
    "s_mov_b32 s15, 0x20000\n"
    "s_mov_b32 s53, 0\n"
    "s_mov_b32 s100, s42\n"
    "s_mov_b32 s101, s43\n"
    "v_xor_b32 v36, 0, v100\n"
    "v_xor_b32 v40, 0, v102\n"
    "v_xor_b32 v37, 64, v100\n"
    "v_xor_b32 v41, 64, v102\n"
    "v_xor_b32 v38, 128, v100\n"
    "v_xor_b32 v42, 128, v102\n"
    "v_xor_b32 v39, 192, v100\n"
    "v_xor_b32 v43, 192, v102\n"
    "s_mov_b32 s12, s24\n"
    "s_mov_b32 s13, s25\n"
    "s_mov_b32 s14, 4718592\n"
    "s_mov_b32 exec_hi, 0xf\n"
    "s_mul_i32 s55, s26, 18432\n"
    "s_add_u32 s55, s55, 65536\n"
    "s_mov_b32 m0, s55\n"
    "s_nop 0\n"
    "buffer_load_dwordx4 v121, s[12:15], s31 offen lds\n"
    "s_add_u32 s31, s31, 1152\n"
    "s_add_u32 s55, s55, 576\n"
    "s_mov_b32 m0, s55\n"
    "s_nop 0\n"
    "buffer_load_dwordx4 v121, s[12:15], s31 offen lds\n"
    "s_add_u32 s31, s31, 1152\n"
    "s_add_u32 s55, s55, 576\n"
    "s_mov_b32 m0, s55\n"
    "s_nop 0\n"
    "buffer_load_dwordx4 v121, s[12:15], s31 offen lds\n"
    "s_add_u32 s31, s31, 1152\n"
    "s_add_u32 s55, s55, 576\n"
    "s_mov_b32 m0, s55\n"
    "s_nop 0\n"
    "buffer_load_dwordx4 v121, s[12:15], s31 offen lds\n"
    "s_add_u32 s31, s31, 1152\n"
    "s_add_u32 s55, s55, 576\n"
    "s_mov_b32 m0, s55\n"
    "s_nop 0\n"
    "buffer_load_dwordx4 v121, s[12:15], s31 offen lds\n"
    "s_add_u32 s31, s31, 1152\n"
    "s_add_u32 s55, s55, 576\n"
    "s_mov_b32 m0, s55\n"
    "s_nop 0\n"
    "buffer_load_dwordx4 v121, s[12:15], s31 offen lds\n"
    "s_add_u32 s31, s31, 1152\n"
    "s_add_u32 s55, s55, 576\n"
    "s_mov_b32 m0, s55\n"
    "s_nop 0\n"
    "buffer_load_dwordx4 v121, s[12:15], s31 offen lds\n"
    "s_add_u32 s31, s31, 1152\n"
    "s_add_u32 s55, s55, 576\n"
    "s_mov_b32 m0, s55\n"
    "s_nop 0\n"
    "buffer_load_dwordx4 v121, s[12:15], s31 offen lds\n"
    "s_add_u32 s31, s31, 1152\n"
    "s_add_u32 s55, s55, 576\n"
    "s_mov_b32 m0, s55\n"
    "s_nop 0\n"
    "buffer_load_dwordx4 v121, s[12:15], s31 offen lds\n"
    "s_add_u32 s31, s31, 1152\n"
    "s_add_u32 s55, s55, 576\n"
    "s_mov_b32 m0, s55\n"
    "s_nop 0\n"
    "buffer_load_dwordx4 v121, s[12:15], s31 offen lds\n"
    "s_add_u32 s31, s31, 1152\n"
    "s_add_u32 s55, s55, 576\n"
    "s_mov_b32 m0, s55\n"
    "s_nop 0\n"
    "buffer_load_dwordx4 v121, s[12:15], s31 offen lds\n"
    "s_add_u32 s31, s31, 1152\n"
    "s_add_u32 s55, s55, 576\n"
    "s_mov_b32 m0, s55\n"
    "s_nop 0\n"
    "buffer_load_dwordx4 v121, s[12:15], s31 offen lds\n"
    "s_add_u32 s31, s31, 1152\n"
    "s_add_u32 s55, s55, 576\n"
    "s_mov_b32 m0, s55\n"
    "s_nop 0\n"
    "buffer_load_dwordx4 v121, s[12:15], s31 offen lds\n"
    "s_add_u32 s31, s31, 1152\n"
    "s_add_u32 s55, s55, 576\n"
    "s_mov_b32 m0, s55\n"
    "s_nop 0\n"
    "buffer_load_dwordx4 v121, s[12:15], s31 offen lds\n"
    "s_add_u32 s31, s31, 1152\n"
    "s_add_u32 s55, s55, 576\n"
    "s_mov_b32 m0, s55\n"
    "s_nop 0\n"
    "buffer_load_dwordx4 v121, s[12:15], s31 offen lds\n"
    "s_add_u32 s31, s31, 1152\n"
    "s_add_u32 s55, s55, 576\n"
    "s_mov_b32 m0, s55\n"
    "s_nop 0\n"
    "buffer_load_dwordx4 v121, s[12:15], s31 offen lds\n"
    "s_add_u32 s31, s31, 1152\n"
    "s_add_u32 s55, s55, 576\n"
    "s_mov_b32 m0, s55\n"
    "s_nop 0\n"
    "buffer_load_dwordx4 v121, s[12:15], s31 offen lds\n"
    "s_add_u32 s31, s31, 1152\n"
    "s_add_u32 s55, s55, 576\n"
    "s_mov_b32 m0, s55\n"
    "s_nop 0\n"
    "buffer_load_dwordx4 v121, s[12:15], s31 offen lds\n"
    "s_add_u32 s31, s31, 1152\n"
    "s_add_u32 s55, s55, 576\n"
    "s_mov_b32 m0, s55\n"
    "s_nop 0\n"
    "buffer_load_dwordx4 v121, s[12:15], s31 offen lds\n"
    "s_add_u32 s31, s31, 1152\n"
    "s_add_u32 s55, s55, 576\n"
    "s_mov_b32 m0, s55\n"
    "s_nop 0\n"
    "buffer_load_dwordx4 v121, s[12:15], s31 offen lds\n"
    "s_add_u32 s31, s31, 1152\n"
    "s_add_u32 s55, s55, 576\n"
    "s_mov_b32 m0, s55\n"
    "s_nop 0\n"
    "buffer_load_dwordx4 v121, s[12:15], s31 offen lds\n"
    "s_add_u32 s31, s31, 1152\n"
    "s_add_u32 s55, s55, 576\n"
    "s_mov_b32 m0, s55\n"
    "s_nop 0\n"
    "buffer_load_dwordx4 v121, s[12:15], s31 offen lds\n"
    "s_add_u32 s31, s31, 1152\n"
    "s_add_u32 s55, s55, 576\n"
    "s_mov_b32 m0, s55\n"
    "s_nop 0\n"
    "buffer_load_dwordx4 v121, s[12:15], s31 offen lds\n"
    "s_add_u32 s31, s31, 1152\n"
    "s_add_u32 s55, s55, 576\n"
    "s_mov_b32 m0, s55\n"
    "s_nop 0\n"
    "buffer_load_dwordx4 v121, s[12:15], s31 offen lds\n"
    "s_add_u32 s31, s31, 1152\n"
    "s_add_u32 s55, s55, 576\n"
    "s_mov_b32 m0, s55\n"
    "s_nop 0\n"
    "buffer_load_dwordx4 v121, s[12:15], s31 offen lds\n"
    "s_add_u32 s31, s31, 1152\n"
    "s_add_u32 s55, s55, 576\n"
    "s_mov_b32 m0, s55\n"
    "s_nop 0\n"
    "buffer_load_dwordx4 v121, s[12:15], s31 offen lds\n"
    "s_add_u32 s31, s31, 1152\n"
    "s_add_u32 s55, s55, 576\n"
    "s_mov_b32 m0, s55\n"
    "s_nop 0\n"
    "buffer_load_dwordx4 v121, s[12:15], s31 offen lds\n"
    "s_add_u32 s31, s31, 1152\n"
    "s_add_u32 s55, s55, 576\n"
    "s_mov_b32 m0, s55\n"
    "s_nop 0\n"
    "buffer_load_dwordx4 v121, s[12:15], s31 offen lds\n"
    "s_add_u32 s31, s31, 1152\n"
    "s_add_u32 s55, s55, 576\n"
    "s_mov_b32 m0, s55\n"
    "s_nop 0\n"
    "buffer_load_dwordx4 v121, s[12:15], s31 offen lds\n"
    "s_add_u32 s31, s31, 1152\n"
    "s_add_u32 s55, s55, 576\n"
    "s_mov_b32 m0, s55\n"
    "s_nop 0\n"
    "buffer_load_dwordx4 v121, s[12:15], s31 offen lds\n"
    "s_add_u32 s31, s31, 1152\n"
    "s_add_u32 s55, s55, 576\n"
    "s_mov_b32 m0, s55\n"
    "s_nop 0\n"
    "buffer_load_dwordx4 v121, s[12:15], s31 offen lds\n"
    "s_add_u32 s31, s31, 1152\n"
    "s_add_u32 s55, s55, 576\n"
    "s_mov_b32 m0, s55\n"
    "s_nop 0\n"
    "buffer_load_dwordx4 v121, s[12:15], s31 offen lds\n"
    "s_add_u32 s31, s31, 1152\n"
    "s_add_u32 s55, s55, 576\n"
    "s_mov_b64 exec, -1\n"
    "s_mov_b32 s12, s18\n"
    "s_mov_b32 s13, s19\n"
    "s_mov_b32 s14, 4718592\n"
    "s_mov_b32 s54, 0\n"
    "s_add_u32 s55, s41, 0\n"
    "s_mov_b32 m0, s55\n"
    "s_nop 0\n"
    "buffer_load_dwordx4 v36, s[12:15], s54 offen lds\n"
    "s_mov_b32 s54, 131072\n"
    "s_add_u32 s55, s41, 4096\n"
    "s_mov_b32 m0, s55\n"
    "s_nop 0\n"
    "buffer_load_dwordx4 v37, s[12:15], s54 offen lds\n"
    "s_mov_b32 s54, 262144\n"
    "s_add_u32 s55, s41, 8192\n"
    "s_mov_b32 m0, s55\n"
    "s_nop 0\n"
    "buffer_load_dwordx4 v38, s[12:15], s54 offen lds\n"
    "s_mov_b32 s54, 393216\n"
    "s_add_u32 s55, s41, 12288\n"
    "s_mov_b32 m0, s55\n"
    "s_nop 0\n"
    "buffer_load_dwordx4 v39, s[12:15], s54 offen lds\n"
    "s_mov_b32 s54, 524288\n"
    "s_add_u32 s55, s41, 16384\n"
    "s_mov_b32 m0, s55\n"
    "s_nop 0\n"
    "buffer_load_dwordx4 v36, s[12:15], s54 offen lds\n"
    "s_mov_b32 s54, 655360\n"
    "s_add_u32 s55, s41, 20480\n"
    "s_mov_b32 m0, s55\n"
    "s_nop 0\n"
    "buffer_load_dwordx4 v37, s[12:15], s54 offen lds\n"
    "s_mov_b32 s54, 786432\n"
    "s_add_u32 s55, s41, 24576\n"
    "s_mov_b32 m0, s55\n"
    "s_nop 0\n"
    "buffer_load_dwordx4 v38, s[12:15], s54 offen lds\n"
    "s_mov_b32 s54, 917504\n"
    "s_add_u32 s55, s41, 28672\n"
    "s_mov_b32 m0, s55\n"
    "s_nop 0\n"
    "buffer_load_dwordx4 v39, s[12:15], s54 offen lds\n"
    "s_mov_b32 s54, 1048576\n"
    "s_add_u32 s55, s41, 32768\n"
    "s_mov_b32 m0, s55\n"
    "s_nop 0\n"
    "buffer_load_dwordx4 v36, s[12:15], s54 offen lds\n"
    "s_mov_b32 s54, 1179648\n"
    "s_add_u32 s55, s41, 36864\n"
    "s_mov_b32 m0, s55\n"
    "s_nop 0\n"
    "buffer_load_dwordx4 v37, s[12:15], s54 offen lds\n"
    "s_mov_b32 s54, 1310720\n"
    "s_add_u32 s55, s41, 40960\n"
    "s_mov_b32 m0, s55\n"
    "s_nop 0\n"
    "buffer_load_dwordx4 v38, s[12:15], s54 offen lds\n"
    "s_mov_b32 s54, 1441792\n"
    "s_add_u32 s55, s41, 45056\n"
    "s_mov_b32 m0, s55\n"
    "s_nop 0\n"
    "buffer_load_dwordx4 v39, s[12:15], s54 offen lds\n"
    "s_mov_b32 s54, 1572864\n"
    "s_add_u32 s55, s41, 49152\n"
    "s_mov_b32 m0, s55\n"
    "s_nop 0\n"
    "buffer_load_dwordx4 v36, s[12:15], s54 offen lds\n"
    "s_mov_b32 s54, 1703936\n"
    "s_add_u32 s55, s41, 53248\n"
    "s_mov_b32 m0, s55\n"
    "s_nop 0\n"
    "buffer_load_dwordx4 v37, s[12:15], s54 offen lds\n"
    "s_mov_b32 s54, 1835008\n"
    "s_add_u32 s55, s41, 57344\n"
    "s_mov_b32 m0, s55\n"
    "s_nop 0\n"
    "buffer_load_dwordx4 v38, s[12:15], s54 offen lds\n"
    "s_mov_b32 s54, 1966080\n"
    "s_add_u32 s55, s41, 61440\n"
    "s_mov_b32 m0, s55\n"
    "s_nop 0\n"
    "buffer_load_dwordx4 v39, s[12:15], s54 offen lds\n"
    "s_waitcnt vmcnt(16)\n"
    "s_waitcnt lgkmcnt(0)\n"
    "s_barrier\n"
    "ds_read_b64_tr_b16 v[176:177], v101\n"
    "ds_read_b64_tr_b16 v[178:179], v101 offset:1152\n"
    "ds_read_b64_tr_b16 v[180:181], v101 offset:4608\n"
    "ds_read_b64_tr_b16 v[182:183], v101 offset:5760\n"
    "ds_read_b64_tr_b16 v[184:185], v101 offset:36864\n"
    "ds_read_b64_tr_b16 v[186:187], v101 offset:38016\n"
    "ds_read_b64_tr_b16 v[188:189], v101 offset:41472\n"
    "ds_read_b64_tr_b16 v[190:191], v101 offset:42624\n"
    "ds_read_b64_tr_b16 v[192:193], v101 offset:32\n"
    "ds_read_b64_tr_b16 v[194:195], v101 offset:1184\n"
    "ds_read_b64_tr_b16 v[196:197], v101 offset:4640\n"
    "ds_read_b64_tr_b16 v[198:199], v101 offset:5792\n"
    "ds_read_b64_tr_b16 v[200:201], v101 offset:36896\n"
    "ds_read_b64_tr_b16 v[202:203], v101 offset:38048\n"
    "ds_read_b64_tr_b16 v[204:205], v101 offset:41504\n"
    "s_waitcnt lgkmcnt(14)\n"
    "ds_read_b64_tr_b16 v[206:207], v101 offset:42656\n"
    "s_waitcnt lgkmcnt(14)\n"
    "ds_read_b64_tr_b16 v[208:209], v101 offset:64\n"
    "s_waitcnt lgkmcnt(14)\n"
    "ds_read_b64_tr_b16 v[210:211], v101 offset:1216\n"
    "s_waitcnt lgkmcnt(14)\n"
    "ds_read_b64_tr_b16 v[212:213], v101 offset:4672\n"
    "s_waitcnt lgkmcnt(14)\n"
    "ds_read_b64_tr_b16 v[214:215], v101 offset:5824\n"
    "s_waitcnt lgkmcnt(14)\n"
    "ds_read_b64_tr_b16 v[216:217], v101 offset:36928\n"
    "s_waitcnt lgkmcnt(14)\n"
    "ds_read_b64_tr_b16 v[218:219], v101 offset:38080\n"
    "s_waitcnt lgkmcnt(14)\n"
    "ds_read_b64_tr_b16 v[220:221], v101 offset:41536\n"
    "s_waitcnt lgkmcnt(14)\n"
    "ds_read_b64_tr_b16 v[222:223], v101 offset:42688\n"
    "s_waitcnt lgkmcnt(14)\n"
    "ds_read_b64_tr_b16 v[224:225], v101 offset:96\n"
    "s_waitcnt lgkmcnt(14)\n"
    "ds_read_b64_tr_b16 v[226:227], v101 offset:1248\n"
    "s_waitcnt lgkmcnt(14)\n"
    "ds_read_b64_tr_b16 v[228:229], v101 offset:4704\n"
    "s_waitcnt lgkmcnt(14)\n"
    "ds_read_b64_tr_b16 v[230:231], v101 offset:5856\n"
    "s_waitcnt lgkmcnt(14)\n"
    "ds_read_b64_tr_b16 v[232:233], v101 offset:36960\n"
    "s_waitcnt lgkmcnt(14)\n"
    "ds_read_b64_tr_b16 v[234:235], v101 offset:38112\n"
    "s_waitcnt lgkmcnt(14)\n"
    "ds_read_b64_tr_b16 v[236:237], v101 offset:41568\n"
    "s_waitcnt lgkmcnt(14)\n"
    "ds_read_b64_tr_b16 v[238:239], v101 offset:42720\n"
    "s_cmp_ge_u32 s26, 2\n"
    "s_cbranch_scc1 XP1\n"
    "s_lshr_b32 s54, s39, 9\n"
    "v_add_u32 v101, s54, v101\n"
    "s_waitcnt lgkmcnt(14)\n"
    "ds_read_b64_tr_b16 v[240:241], v101\n"
    "s_waitcnt lgkmcnt(14)\n"
    "ds_read_b64_tr_b16 v[242:243], v101 offset:1152\n"
    "s_waitcnt lgkmcnt(14)\n"
    "ds_read_b64_tr_b16 v[244:245], v101 offset:4608\n"
    "s_waitcnt lgkmcnt(14)\n"
    "ds_read_b64_tr_b16 v[246:247], v101 offset:5760\n"
    "s_waitcnt lgkmcnt(14)\n"
    "ds_read_b64_tr_b16 v[248:249], v101 offset:36864\n"
    "s_waitcnt lgkmcnt(14)\n"
    "ds_read_b64_tr_b16 v[250:251], v101 offset:38016\n"
    "s_waitcnt lgkmcnt(14)\n"
    "ds_read_b64_tr_b16 v[252:253], v101 offset:41472\n"
    "s_waitcnt lgkmcnt(14)\n"
    "ds_read_b64_tr_b16 v[254:255], v101 offset:42624\n"
    "s_waitcnt lgkmcnt(0)\n"
    "XP1:\n"
    "s_waitcnt lgkmcnt(0)\n"
    "s_barrier\n"
    "s_mov_b32 s54, 2359296\n"
    "s_add_u32 s55, s41, 65536\n"
    "s_mov_b32 m0, s55\n"
    "s_nop 0\n"
    "buffer_load_dwordx4 v36, s[12:15], s54 offen lds\n"
    "s_mov_b32 s54, 2490368\n"
    "s_add_u32 s55, s41, 69632\n"
    "s_mov_b32 m0, s55\n"
    "s_nop 0\n"
    "buffer_load_dwordx4 v37, s[12:15], s54 offen lds\n"
    "s_mov_b32 s54, 2621440\n"
    "s_add_u32 s55, s41, 73728\n"
    "s_mov_b32 m0, s55\n"
    "s_nop 0\n"
    "buffer_load_dwordx4 v38, s[12:15], s54 offen lds\n"
    "s_mov_b32 s54, 2752512\n"
    "s_add_u32 s55, s41, 77824\n"
    "s_mov_b32 m0, s55\n"
    "s_nop 0\n"
    "buffer_load_dwordx4 v39, s[12:15], s54 offen lds\n"
    "s_mov_b32 s54, 2883584\n"
    "s_add_u32 s55, s41, 81920\n"
    "s_mov_b32 m0, s55\n"
    "s_nop 0\n"
    "buffer_load_dwordx4 v36, s[12:15], s54 offen lds\n"
    "s_mov_b32 s54, 3014656\n"
    "s_add_u32 s55, s41, 86016\n"
    "s_mov_b32 m0, s55\n"
    "s_nop 0\n"
    "buffer_load_dwordx4 v37, s[12:15], s54 offen lds\n"
    "s_mov_b32 s54, 3145728\n"
    "s_add_u32 s55, s41, 90112\n"
    "s_mov_b32 m0, s55\n"
    "s_nop 0\n"
    "buffer_load_dwordx4 v38, s[12:15], s54 offen lds\n"
    "s_mov_b32 s54, 3276800\n"
    "s_add_u32 s55, s41, 94208\n"
    "s_mov_b32 m0, s55\n"
    "s_nop 0\n"
    "buffer_load_dwordx4 v39, s[12:15], s54 offen lds\n"
    "s_mov_b32 s54, 3407872\n"
    "s_add_u32 s55, s41, 98304\n"
    "s_mov_b32 m0, s55\n"
    "s_nop 0\n"
    "buffer_load_dwordx4 v36, s[12:15], s54 offen lds\n"
    "s_mov_b32 s54, 3538944\n"
    "s_add_u32 s55, s41, 102400\n"
    "s_mov_b32 m0, s55\n"
    "s_nop 0\n"
    "buffer_load_dwordx4 v37, s[12:15], s54 offen lds\n"
    "s_mov_b32 s54, 3670016\n"
    "s_add_u32 s55, s41, 106496\n"
    "s_mov_b32 m0, s55\n"
    "s_nop 0\n"
    "buffer_load_dwordx4 v38, s[12:15], s54 offen lds\n"
    "s_mov_b32 s54, 3801088\n"
    "s_add_u32 s55, s41, 110592\n"
    "s_mov_b32 m0, s55\n"
    "s_nop 0\n"
    "buffer_load_dwordx4 v39, s[12:15], s54 offen lds\n"
    "s_mov_b32 s54, 3932160\n"
    "s_add_u32 s55, s41, 114688\n"
    "s_mov_b32 m0, s55\n"
    "s_nop 0\n"
    "buffer_load_dwordx4 v36, s[12:15], s54 offen lds\n"
    "s_mov_b32 s54, 4063232\n"
    "s_add_u32 s55, s41, 118784\n"
    "s_mov_b32 m0, s55\n"
    "s_nop 0\n"
    "buffer_load_dwordx4 v37, s[12:15], s54 offen lds\n"
    "s_mov_b32 s54, 4194304\n"
    "s_add_u32 s55, s41, 122880\n"
    "s_mov_b32 m0, s55\n"
    "s_nop 0\n"
    "buffer_load_dwordx4 v38, s[12:15], s54 offen lds\n"
    "s_mov_b32 s54, 4325376\n"
    "s_add_u32 s55, s41, 126976\n"
    "s_mov_b32 m0, s55\n"
    "s_nop 0\n"
    "buffer_load_dwordx4 v39, s[12:15], s54 offen lds\n"
    "s_mov_b32 s54, 0\n"
    "s_add_u32 s55, s41, 131072\n"
    "s_mov_b32 m0, s55\n"
    "s_nop 0\n"
    "buffer_load_dwordx4 v40, s[12:15], s54 offen lds\n"
    "s_mov_b32 s54, 131072\n"
    "s_add_u32 s55, s41, 135168\n"
    "s_mov_b32 m0, s55\n"
    "s_nop 0\n"
    "buffer_load_dwordx4 v41, s[12:15], s54 offen lds\n"
    "s_mov_b32 s54, 8192\n"
    "s_add_u32 s55, s41, 139264\n"
    "s_mov_b32 m0, s55\n"
    "s_nop 0\n"
    "buffer_load_dwordx4 v42, s[12:15], s54 offen lds\n"
    "s_mov_b32 s54, 139264\n"
    "s_add_u32 s55, s41, 143360\n"
    "s_mov_b32 m0, s55\n"
    "s_nop 0\n"
    "buffer_load_dwordx4 v43, s[12:15], s54 offen lds\n"
    "s_waitcnt vmcnt(32)\n"
    "s_waitcnt lgkmcnt(0)\n"
    "s_barrier\n"
    "v_add_u32 v108, 0, v104\n"
    "v_add_u32 v109, 0, v105\n"
    "ds_read_b64_tr_b16 a[0:1], v108\n"
    "ds_read_b64_tr_b16 a[2:3], v108 offset:256\n"
    "ds_read_b64_tr_b16 a[4:5], v108 offset:1024\n"
    "ds_read_b64_tr_b16 a[6:7], v108 offset:1280\n"
    "ds_read_b64_tr_b16 a[72:73], v109\n"
    "ds_read_b64_tr_b16 a[74:75], v109 offset:256\n"
    "ds_read_b64_tr_b16 a[76:77], v109 offset:1024\n"
    "ds_read_b64_tr_b16 a[78:79], v109 offset:1280\n"
    "s_waitcnt vmcnt(28)\n"
    "s_waitcnt lgkmcnt(0)\n"
    "s_barrier\n"
    "s_mov_b32 s12, s20\n"
    "s_mov_b32 s13, s21\n"
    "s_mov_b32 s14, 4718592\n"
    "s_mov_b32 s54, 0\n"
    "s_add_u32 s55, s41, 0\n"
    "s_mov_b32 m0, s55\n"
    "s_nop 0\n"
    "buffer_load_dwordx4 v36, s[12:15], s54 offen lds\n"
    "s_mov_b32 s54, 131072\n"
    "s_add_u32 s55, s41, 4096\n"
    "s_mov_b32 m0, s55\n"
    "s_nop 0\n"
    "buffer_load_dwordx4 v37, s[12:15], s54 offen lds\n"
    "s_mov_b32 s54, 262144\n"
    "s_add_u32 s55, s41, 8192\n"
    "s_mov_b32 m0, s55\n"
    "s_nop 0\n"
    "buffer_load_dwordx4 v38, s[12:15], s54 offen lds\n"
    "s_mov_b32 s54, 393216\n"
    "s_add_u32 s55, s41, 12288\n"
    "s_mov_b32 m0, s55\n"
    "s_nop 0\n"
    "buffer_load_dwordx4 v39, s[12:15], s54 offen lds\n"
    "v_add_u32 v108, 16384, v104\n"
    "v_add_u32 v109, 16384, v105\n"
    "ds_read_b64_tr_b16 a[8:9], v108\n"
    "ds_read_b64_tr_b16 a[10:11], v108 offset:256\n"
    "ds_read_b64_tr_b16 a[12:13], v108 offset:1024\n"
    "ds_read_b64_tr_b16 a[14:15], v108 offset:1280\n"
    "ds_read_b64_tr_b16 a[80:81], v109\n"
    "ds_read_b64_tr_b16 a[82:83], v109 offset:256\n"
    "ds_read_b64_tr_b16 a[84:85], v109 offset:1024\n"
    "ds_read_b64_tr_b16 a[86:87], v109 offset:1280\n"
    "s_waitcnt vmcnt(28)\n"
    "s_waitcnt lgkmcnt(0)\n"
    "s_barrier\n"
    "s_mov_b32 s54, 524288\n"
    "s_add_u32 s55, s41, 16384\n"
    "s_mov_b32 m0, s55\n"
    "s_nop 0\n"
    "buffer_load_dwordx4 v36, s[12:15], s54 offen lds\n"
    "s_mov_b32 s54, 655360\n"
    "s_add_u32 s55, s41, 20480\n"
    "s_mov_b32 m0, s55\n"
    "s_nop 0\n"
    "buffer_load_dwordx4 v37, s[12:15], s54 offen lds\n"
    "s_mov_b32 s54, 786432\n"
    "s_add_u32 s55, s41, 24576\n"
    "s_mov_b32 m0, s55\n"
    "s_nop 0\n"
    "buffer_load_dwordx4 v38, s[12:15], s54 offen lds\n"
    "s_mov_b32 s54, 917504\n"
    "s_add_u32 s55, s41, 28672\n"
    "s_mov_b32 m0, s55\n"
    "s_nop 0\n"
    "buffer_load_dwordx4 v39, s[12:15], s54 offen lds\n"
    "v_add_u32 v108, 32768, v104\n"
    "v_add_u32 v109, 32768, v105\n"
    "ds_read_b64_tr_b16 a[16:17], v108\n"
    "ds_read_b64_tr_b16 a[18:19], v108 offset:256\n"
    "ds_read_b64_tr_b16 a[20:21], v108 offset:1024\n"
    "ds_read_b64_tr_b16 a[22:23], v108 offset:1280\n"
    "ds_read_b64_tr_b16 a[88:89], v109\n"
    "ds_read_b64_tr_b16 a[90:91], v109 offset:256\n"
    "ds_read_b64_tr_b16 a[92:93], v109 offset:1024\n"
    "ds_read_b64_tr_b16 a[94:95], v109 offset:1280\n"
    "s_waitcnt vmcnt(28)\n"
    "s_waitcnt lgkmcnt(0)\n"
    "s_barrier\n"
    "s_mov_b32 s54, 1048576\n"
    "s_add_u32 s55, s41, 32768\n"
    "s_mov_b32 m0, s55\n"
    "s_nop 0\n"
    "buffer_load_dwordx4 v36, s[12:15], s54 offen lds\n"
    "s_mov_b32 s54, 1179648\n"
    "s_add_u32 s55, s41, 36864\n"
    "s_mov_b32 m0, s55\n"
    "s_nop 0\n"
    "buffer_load_dwordx4 v37, s[12:15], s54 offen lds\n"
    "s_mov_b32 s54, 1310720\n"
    "s_add_u32 s55, s41, 40960\n"
    "s_mov_b32 m0, s55\n"
    "s_nop 0\n"
    "buffer_load_dwordx4 v38, s[12:15], s54 offen lds\n"
    "s_mov_b32 s54, 1441792\n"
    "s_add_u32 s55, s41, 45056\n"
    "s_mov_b32 m0, s55\n"
    "s_nop 0\n"
    "buffer_load_dwordx4 v39, s[12:15], s54 offen lds\n"
    "v_add_u32 v108, 49152, v104\n"
    "v_add_u32 v109, 49152, v105\n"
    "ds_read_b64_tr_b16 a[24:25], v108\n"
    "ds_read_b64_tr_b16 a[26:27], v108 offset:256\n"
    "ds_read_b64_tr_b16 a[28:29], v108 offset:1024\n"
    "ds_read_b64_tr_b16 a[30:31], v108 offset:1280\n"
    "ds_read_b64_tr_b16 a[96:97], v109\n"
    "ds_read_b64_tr_b16 a[98:99], v109 offset:256\n"
    "ds_read_b64_tr_b16 a[100:101], v109 offset:1024\n"
    "ds_read_b64_tr_b16 a[102:103], v109 offset:1280\n"
    "s_waitcnt vmcnt(28)\n"
    "s_waitcnt lgkmcnt(0)\n"
    "s_barrier\n"
    "s_mov_b32 s54, 1572864\n"
    "s_add_u32 s55, s41, 49152\n"
    "s_mov_b32 m0, s55\n"
    "s_nop 0\n"
    "buffer_load_dwordx4 v36, s[12:15], s54 offen lds\n"
    "s_mov_b32 s54, 1703936\n"
    "s_add_u32 s55, s41, 53248\n"
    "s_mov_b32 m0, s55\n"
    "s_nop 0\n"
    "buffer_load_dwordx4 v37, s[12:15], s54 offen lds\n"
    "s_mov_b32 s54, 1835008\n"
    "s_add_u32 s55, s41, 57344\n"
    "s_mov_b32 m0, s55\n"
    "s_nop 0\n"
    "buffer_load_dwordx4 v38, s[12:15], s54 offen lds\n"
    "s_mov_b32 s54, 1966080\n"
    "s_add_u32 s55, s41, 61440\n"
    "s_mov_b32 m0, s55\n"
    "s_nop 0\n"
    "buffer_load_dwordx4 v39, s[12:15], s54 offen lds\n"
    "v_add_u32 v108, 65536, v104\n"
    "v_add_u32 v109, 65536, v105\n"
    "ds_read_b64_tr_b16 a[32:33], v108\n"
    "ds_read_b64_tr_b16 a[34:35], v108 offset:256\n"
    "ds_read_b64_tr_b16 a[36:37], v108 offset:1024\n"
    "ds_read_b64_tr_b16 a[38:39], v108 offset:1280\n"
    "ds_read_b64_tr_b16 a[104:105], v109\n"
    "ds_read_b64_tr_b16 a[106:107], v109 offset:256\n"
    "ds_read_b64_tr_b16 a[108:109], v109 offset:1024\n"
    "ds_read_b64_tr_b16 a[110:111], v109 offset:1280\n"
    "s_waitcnt vmcnt(28)\n"
    "s_waitcnt lgkmcnt(0)\n"
    "s_barrier\n"
    "s_mov_b32 s54, 2359296\n"
    "s_add_u32 s55, s41, 65536\n"
    "s_mov_b32 m0, s55\n"
    "s_nop 0\n"
    "buffer_load_dwordx4 v36, s[12:15], s54 offen lds\n"
    "s_mov_b32 s54, 2490368\n"
    "s_add_u32 s55, s41, 69632\n"
    "s_mov_b32 m0, s55\n"
    "s_nop 0\n"
    "buffer_load_dwordx4 v37, s[12:15], s54 offen lds\n"
    "s_mov_b32 s54, 2621440\n"
    "s_add_u32 s55, s41, 73728\n"
    "s_mov_b32 m0, s55\n"
    "s_nop 0\n"
    "buffer_load_dwordx4 v38, s[12:15], s54 offen lds\n"
    "s_mov_b32 s54, 2752512\n"
    "s_add_u32 s55, s41, 77824\n"
    "s_mov_b32 m0, s55\n"
    "s_nop 0\n"
    "buffer_load_dwordx4 v39, s[12:15], s54 offen lds\n"
    "v_add_u32 v108, 81920, v104\n"
    "v_add_u32 v109, 81920, v105\n"
    "ds_read_b64_tr_b16 a[40:41], v108\n"
    "ds_read_b64_tr_b16 a[42:43], v108 offset:256\n"
    "ds_read_b64_tr_b16 a[44:45], v108 offset:1024\n"
    "ds_read_b64_tr_b16 a[46:47], v108 offset:1280\n"
    "ds_read_b64_tr_b16 a[112:113], v109\n"
    "ds_read_b64_tr_b16 a[114:115], v109 offset:256\n"
    "ds_read_b64_tr_b16 a[116:117], v109 offset:1024\n"
    "ds_read_b64_tr_b16 a[118:119], v109 offset:1280\n"
    "s_waitcnt vmcnt(28)\n"
    "s_waitcnt lgkmcnt(0)\n"
    "s_barrier\n"
    "s_mov_b32 s12, s22\n"
    "s_mov_b32 s13, s23\n"
    "s_mov_b32 s14, 4718592\n"
    "s_mov_b32 s54, 0\n"
    "s_add_u32 s55, s41, 81920\n"
    "s_mov_b32 m0, s55\n"
    "s_nop 0\n"
    "buffer_load_dwordx4 v36, s[12:15], s54 offen lds\n"
    "s_mov_b32 s54, 131072\n"
    "s_add_u32 s55, s41, 86016\n"
    "s_mov_b32 m0, s55\n"
    "s_nop 0\n"
    "buffer_load_dwordx4 v37, s[12:15], s54 offen lds\n"
    "s_mov_b32 s54, 262144\n"
    "s_add_u32 s55, s41, 90112\n"
    "s_mov_b32 m0, s55\n"
    "s_nop 0\n"
    "buffer_load_dwordx4 v38, s[12:15], s54 offen lds\n"
    "s_mov_b32 s54, 393216\n"
    "s_add_u32 s55, s41, 94208\n"
    "s_mov_b32 m0, s55\n"
    "s_nop 0\n"
    "buffer_load_dwordx4 v39, s[12:15], s54 offen lds\n"
    "v_add_u32 v108, 98304, v104\n"
    "v_add_u32 v109, 98304, v105\n"
    "ds_read_b64_tr_b16 a[48:49], v108\n"
    "ds_read_b64_tr_b16 a[50:51], v108 offset:256\n"
    "ds_read_b64_tr_b16 a[52:53], v108 offset:1024\n"
    "ds_read_b64_tr_b16 a[54:55], v108 offset:1280\n"
    "ds_read_b64_tr_b16 a[120:121], v109\n"
    "ds_read_b64_tr_b16 a[122:123], v109 offset:256\n"
    "ds_read_b64_tr_b16 a[124:125], v109 offset:1024\n"
    "ds_read_b64_tr_b16 a[126:127], v109 offset:1280\n"
    "s_waitcnt vmcnt(28)\n"
    "s_waitcnt lgkmcnt(0)\n"
    "s_barrier\n"
    "s_mov_b32 s54, 524288\n"
    "s_add_u32 s55, s41, 98304\n"
    "s_mov_b32 m0, s55\n"
    "s_nop 0\n"
    "buffer_load_dwordx4 v36, s[12:15], s54 offen lds\n"
    "s_mov_b32 s54, 655360\n"
    "s_add_u32 s55, s41, 102400\n"
    "s_mov_b32 m0, s55\n"
    "s_nop 0\n"
    "buffer_load_dwordx4 v37, s[12:15], s54 offen lds\n"
    "s_mov_b32 s54, 786432\n"
    "s_add_u32 s55, s41, 106496\n"
    "s_mov_b32 m0, s55\n"
    "s_nop 0\n"
    "buffer_load_dwordx4 v38, s[12:15], s54 offen lds\n"
    "s_mov_b32 s54, 917504\n"
    "s_add_u32 s55, s41, 110592\n"
    "s_mov_b32 m0, s55\n"
    "s_nop 0\n"
    "buffer_load_dwordx4 v39, s[12:15], s54 offen lds\n"
    "v_add_u32 v108, 114688, v104\n"
    "v_add_u32 v109, 114688, v105\n"
    "ds_read_b64_tr_b16 a[56:57], v108\n"
    "ds_read_b64_tr_b16 a[58:59], v108 offset:256\n"
    "ds_read_b64_tr_b16 a[60:61], v108 offset:1024\n"
    "ds_read_b64_tr_b16 a[62:63], v108 offset:1280\n"
    "ds_read_b64_tr_b16 a[128:129], v109\n"
    "ds_read_b64_tr_b16 a[130:131], v109 offset:256\n"
    "ds_read_b64_tr_b16 a[132:133], v109 offset:1024\n"
    "ds_read_b64_tr_b16 a[134:135], v109 offset:1280\n"
    "s_waitcnt vmcnt(28)\n"
    "s_waitcnt lgkmcnt(0)\n"
    "s_barrier\n"
    "s_mov_b32 s54, 1048576\n"
    "s_add_u32 s55, s41, 114688\n"
    "s_mov_b32 m0, s55\n"
    "s_nop 0\n"
    "buffer_load_dwordx4 v36, s[12:15], s54 offen lds\n"
    "s_mov_b32 s54, 1179648\n"
    "s_add_u32 s55, s41, 118784\n"
    "s_mov_b32 m0, s55\n"
    "s_nop 0\n"
    "buffer_load_dwordx4 v37, s[12:15], s54 offen lds\n"
    "s_mov_b32 s54, 1310720\n"
    "s_add_u32 s55, s41, 122880\n"
    "s_mov_b32 m0, s55\n"
    "s_nop 0\n"
    "buffer_load_dwordx4 v38, s[12:15], s54 offen lds\n"
    "s_mov_b32 s54, 1441792\n"
    "s_add_u32 s55, s41, 126976\n"
    "s_mov_b32 m0, s55\n"
    "s_nop 0\n"
    "buffer_load_dwordx4 v39, s[12:15], s54 offen lds\n"
    "v_add_u32 v108, 131072, v104\n"
    "v_add_u32 v109, 131072, v105\n"
    "ds_read_b64_tr_b16 a[64:65], v108\n"
    "ds_read_b64_tr_b16 a[66:67], v108 offset:256\n"
    "ds_read_b64_tr_b16 a[68:69], v108 offset:1024\n"
    "ds_read_b64_tr_b16 a[70:71], v108 offset:1280\n"
    "ds_read_b64_tr_b16 a[136:137], v109\n"
    "ds_read_b64_tr_b16 a[138:139], v109 offset:256\n"
    "ds_read_b64_tr_b16 a[140:141], v109 offset:1024\n"
    "ds_read_b64_tr_b16 a[142:143], v109 offset:1280\n"
    "s_waitcnt vmcnt(28)\n"
    "s_waitcnt lgkmcnt(0)\n"
    "s_barrier\n"
    "s_mov_b32 s54, 1572864\n"
    "s_add_u32 s55, s41, 131072\n"
    "s_mov_b32 m0, s55\n"
    "s_nop 0\n"
    "buffer_load_dwordx4 v36, s[12:15], s54 offen lds\n"
    "s_mov_b32 s54, 1703936\n"
    "s_add_u32 s55, s41, 135168\n"
    "s_mov_b32 m0, s55\n"
    "s_nop 0\n"
    "buffer_load_dwordx4 v37, s[12:15], s54 offen lds\n"
    "s_mov_b32 s54, 1835008\n"
    "s_add_u32 s55, s41, 139264\n"
    "s_mov_b32 m0, s55\n"
    "s_nop 0\n"
    "buffer_load_dwordx4 v38, s[12:15], s54 offen lds\n"
    "s_mov_b32 s54, 1966080\n"
    "s_add_u32 s55, s41, 143360\n"
    "s_mov_b32 m0, s55\n"
    "s_nop 0\n"
    "buffer_load_dwordx4 v39, s[12:15], s54 offen lds\n"
    "v_add_u32 v108, 0, v104\n"
    "v_add_u32 v109, 0, v105\n"
    "ds_read_b64_tr_b16 a[144:145], v108\n"
    "ds_read_b64_tr_b16 a[146:147], v108 offset:256\n"
    "ds_read_b64_tr_b16 a[148:149], v108 offset:1024\n"
    "ds_read_b64_tr_b16 a[150:151], v108 offset:1280\n"
    "ds_read_b64_tr_b16 a[184:185], v109\n"
    "ds_read_b64_tr_b16 a[186:187], v109 offset:256\n"
    "ds_read_b64_tr_b16 a[188:189], v109 offset:1024\n"
    "ds_read_b64_tr_b16 a[190:191], v109 offset:1280\n"
    "s_waitcnt vmcnt(28)\n"
    "s_waitcnt lgkmcnt(0)\n"
    "s_barrier\n"
    "s_mov_b32 s12, s20\n"
    "s_mov_b32 s13, s21\n"
    "s_mov_b32 s14, 4718592\n"
    "s_mov_b32 s54, 2883584\n"
    "s_add_u32 s55, s41, 0\n"
    "s_mov_b32 m0, s55\n"
    "s_nop 0\n"
    "buffer_load_dwordx4 v36, s[12:15], s54 offen lds\n"
    "s_mov_b32 s54, 3014656\n"
    "s_add_u32 s55, s41, 4096\n"
    "s_mov_b32 m0, s55\n"
    "s_nop 0\n"
    "buffer_load_dwordx4 v37, s[12:15], s54 offen lds\n"
    "s_mov_b32 s54, 3145728\n"
    "s_add_u32 s55, s41, 8192\n"
    "s_mov_b32 m0, s55\n"
    "s_nop 0\n"
    "buffer_load_dwordx4 v38, s[12:15], s54 offen lds\n"
    "s_mov_b32 s54, 3276800\n"
    "s_add_u32 s55, s41, 12288\n"
    "s_mov_b32 m0, s55\n"
    "s_nop 0\n"
    "buffer_load_dwordx4 v39, s[12:15], s54 offen lds\n"
    "v_add_u32 v108, 16384, v104\n"
    "v_add_u32 v109, 16384, v105\n"
    "ds_read_b64_tr_b16 a[152:153], v108\n"
    "ds_read_b64_tr_b16 a[154:155], v108 offset:256\n"
    "ds_read_b64_tr_b16 a[156:157], v108 offset:1024\n"
    "ds_read_b64_tr_b16 a[158:159], v108 offset:1280\n"
    "ds_read_b64_tr_b16 a[192:193], v109\n"
    "ds_read_b64_tr_b16 a[194:195], v109 offset:256\n"
    "ds_read_b64_tr_b16 a[196:197], v109 offset:1024\n"
    "ds_read_b64_tr_b16 a[198:199], v109 offset:1280\n"
    "s_waitcnt vmcnt(28)\n"
    "s_waitcnt lgkmcnt(0)\n"
    "s_barrier\n"
    "s_mov_b32 s54, 3407872\n"
    "s_add_u32 s55, s41, 16384\n"
    "s_mov_b32 m0, s55\n"
    "s_nop 0\n"
    "buffer_load_dwordx4 v36, s[12:15], s54 offen lds\n"
    "s_mov_b32 s54, 3538944\n"
    "s_add_u32 s55, s41, 20480\n"
    "s_mov_b32 m0, s55\n"
    "s_nop 0\n"
    "buffer_load_dwordx4 v37, s[12:15], s54 offen lds\n"
    "s_mov_b32 s54, 3670016\n"
    "s_add_u32 s55, s41, 24576\n"
    "s_mov_b32 m0, s55\n"
    "s_nop 0\n"
    "buffer_load_dwordx4 v38, s[12:15], s54 offen lds\n"
    "s_mov_b32 s54, 3801088\n"
    "s_add_u32 s55, s41, 28672\n"
    "s_mov_b32 m0, s55\n"
    "s_nop 0\n"
    "buffer_load_dwordx4 v39, s[12:15], s54 offen lds\n"
    "v_add_u32 v108, 32768, v104\n"
    "v_add_u32 v109, 32768, v105\n"
    "ds_read_b64_tr_b16 a[160:161], v108\n"
    "ds_read_b64_tr_b16 a[162:163], v108 offset:256\n"
    "ds_read_b64_tr_b16 a[164:165], v108 offset:1024\n"
    "ds_read_b64_tr_b16 a[166:167], v108 offset:1280\n"
    "ds_read_b64_tr_b16 a[200:201], v109\n"
    "ds_read_b64_tr_b16 a[202:203], v109 offset:256\n"
    "ds_read_b64_tr_b16 a[204:205], v109 offset:1024\n"
    "ds_read_b64_tr_b16 a[206:207], v109 offset:1280\n"
    "s_waitcnt vmcnt(28)\n"
    "s_waitcnt lgkmcnt(0)\n"
    "s_barrier\n"
    "s_mov_b32 s54, 3932160\n"
    "s_add_u32 s55, s41, 32768\n"
    "s_mov_b32 m0, s55\n"
    "s_nop 0\n"
    "buffer_load_dwordx4 v36, s[12:15], s54 offen lds\n"
    "s_mov_b32 s54, 4063232\n"
    "s_add_u32 s55, s41, 36864\n"
    "s_mov_b32 m0, s55\n"
    "s_nop 0\n"
    "buffer_load_dwordx4 v37, s[12:15], s54 offen lds\n"
    "s_mov_b32 s54, 4194304\n"
    "s_add_u32 s55, s41, 40960\n"
    "s_mov_b32 m0, s55\n"
    "s_nop 0\n"
    "buffer_load_dwordx4 v38, s[12:15], s54 offen lds\n"
    "s_mov_b32 s54, 4325376\n"
    "s_add_u32 s55, s41, 45056\n"
    "s_mov_b32 m0, s55\n"
    "s_nop 0\n"
    "buffer_load_dwordx4 v39, s[12:15], s54 offen lds\n"
    "v_add_u32 v108, 49152, v104\n"
    "v_add_u32 v109, 49152, v105\n"
    "ds_read_b64_tr_b16 a[168:169], v108\n"
    "ds_read_b64_tr_b16 a[170:171], v108 offset:256\n"
    "ds_read_b64_tr_b16 a[172:173], v108 offset:1024\n"
    "ds_read_b64_tr_b16 a[174:175], v108 offset:1280\n"
    "ds_read_b64_tr_b16 a[208:209], v109\n"
    "ds_read_b64_tr_b16 a[210:211], v109 offset:256\n"
    "ds_read_b64_tr_b16 a[212:213], v109 offset:1024\n"
    "ds_read_b64_tr_b16 a[214:215], v109 offset:1280\n"
    "s_waitcnt vmcnt(28)\n"
    "s_waitcnt lgkmcnt(0)\n"
    "s_barrier\n"
    "s_mov_b32 s54, 0\n"
    "s_add_u32 s55, s41, 49152\n"
    "s_mov_b32 m0, s55\n"
    "s_nop 0\n"
    "buffer_load_dwordx4 v40, s[12:15], s54 offen lds\n"
    "s_mov_b32 s54, 131072\n"
    "s_add_u32 s55, s41, 53248\n"
    "s_mov_b32 m0, s55\n"
    "s_nop 0\n"
    "buffer_load_dwordx4 v41, s[12:15], s54 offen lds\n"
    "s_mov_b32 s54, 8192\n"
    "s_add_u32 s55, s41, 57344\n"
    "s_mov_b32 m0, s55\n"
    "s_nop 0\n"
    "buffer_load_dwordx4 v42, s[12:15], s54 offen lds\n"
    "s_mov_b32 s54, 139264\n"
    "s_add_u32 s55, s41, 61440\n"
    "s_mov_b32 m0, s55\n"
    "s_nop 0\n"
    "buffer_load_dwordx4 v43, s[12:15], s54 offen lds\n"
    "v_add_u32 v108, 65536, v104\n"
    "v_add_u32 v109, 65536, v105\n"
    "ds_read_b64_tr_b16 a[176:177], v108\n"
    "ds_read_b64_tr_b16 a[178:179], v108 offset:256\n"
    "ds_read_b64_tr_b16 a[180:181], v108 offset:1024\n"
    "ds_read_b64_tr_b16 a[182:183], v108 offset:1280\n"
    "ds_read_b64_tr_b16 a[216:217], v109\n"
    "ds_read_b64_tr_b16 a[218:219], v109 offset:256\n"
    "ds_read_b64_tr_b16 a[220:221], v109 offset:1024\n"
    "ds_read_b64_tr_b16 a[222:223], v109 offset:1280\n"
    "s_waitcnt vmcnt(28)\n"
    "s_waitcnt lgkmcnt(0)\n"
    "s_barrier\n"
    "s_mov_b32 s12, s22\n"
    "s_mov_b32 s13, s23\n"
    "s_mov_b32 s14, 4718592\n"
    "s_mov_b32 s54, 2359296\n"
    "s_add_u32 s55, s41, 65536\n"
    "s_mov_b32 m0, s55\n"
    "s_nop 0\n"
    "buffer_load_dwordx4 v36, s[12:15], s54 offen lds\n"
    "s_mov_b32 s54, 2490368\n"
    "s_add_u32 s55, s41, 69632\n"
    "s_mov_b32 m0, s55\n"
    "s_nop 0\n"
    "buffer_load_dwordx4 v37, s[12:15], s54 offen lds\n"
    "s_mov_b32 s54, 2621440\n"
    "s_add_u32 s55, s41, 73728\n"
    "s_mov_b32 m0, s55\n"
    "s_nop 0\n"
    "buffer_load_dwordx4 v38, s[12:15], s54 offen lds\n"
    "s_mov_b32 s54, 2752512\n"
    "s_add_u32 s55, s41, 77824\n"
    "s_mov_b32 m0, s55\n"
    "s_nop 0\n"
    "buffer_load_dwordx4 v39, s[12:15], s54 offen lds\n"
    "v_add_u32 v108, 81920, v104\n"
    "v_add_u32 v109, 81920, v105\n"
    "ds_read_b64_tr_b16 a[224:225], v108\n"
    "ds_read_b64_tr_b16 a[226:227], v108 offset:256\n"
    "ds_read_b64_tr_b16 a[228:229], v108 offset:1024\n"
    "ds_read_b64_tr_b16 a[230:231], v108 offset:1280\n"
    "ds_read_b64_tr_b16 v[144:145], v109\n"
    "ds_read_b64_tr_b16 v[146:147], v109 offset:256\n"
    "ds_read_b64_tr_b16 v[148:149], v109 offset:1024\n"
    "ds_read_b64_tr_b16 v[150:151], v109 offset:1280\n"
    "s_waitcnt vmcnt(28)\n"
    "s_waitcnt lgkmcnt(0)\n"
    "s_barrier\n"
    "s_mov_b32 s54, 2883584\n"
    "s_add_u32 s55, s41, 81920\n"
    "s_mov_b32 m0, s55\n"
    "s_nop 0\n"
    "buffer_load_dwordx4 v36, s[12:15], s54 offen lds\n"
    "s_mov_b32 s54, 3014656\n"
    "s_add_u32 s55, s41, 86016\n"
    "s_mov_b32 m0, s55\n"
    "s_nop 0\n"
    "buffer_load_dwordx4 v37, s[12:15], s54 offen lds\n"
    "s_mov_b32 s54, 3145728\n"
    "s_add_u32 s55, s41, 90112\n"
    "s_mov_b32 m0, s55\n"
    "s_nop 0\n"
    "buffer_load_dwordx4 v38, s[12:15], s54 offen lds\n"
    "s_mov_b32 s54, 3276800\n"
    "s_add_u32 s55, s41, 94208\n"
    "s_mov_b32 m0, s55\n"
    "s_nop 0\n"
    "buffer_load_dwordx4 v39, s[12:15], s54 offen lds\n"
    "v_add_u32 v108, 98304, v104\n"
    "v_add_u32 v109, 98304, v105\n"
    "ds_read_b64_tr_b16 a[232:233], v108\n"
    "ds_read_b64_tr_b16 a[234:235], v108 offset:256\n"
    "ds_read_b64_tr_b16 a[236:237], v108 offset:1024\n"
    "ds_read_b64_tr_b16 a[238:239], v108 offset:1280\n"
    "ds_read_b64_tr_b16 v[152:153], v109\n"
    "ds_read_b64_tr_b16 v[154:155], v109 offset:256\n"
    "ds_read_b64_tr_b16 v[156:157], v109 offset:1024\n"
    "ds_read_b64_tr_b16 v[158:159], v109 offset:1280\n"
    "s_waitcnt vmcnt(28)\n"
    "s_waitcnt lgkmcnt(0)\n"
    "s_barrier\n"
    "s_mov_b32 s54, 3407872\n"
    "s_add_u32 s55, s41, 98304\n"
    "s_mov_b32 m0, s55\n"
    "s_nop 0\n"
    "buffer_load_dwordx4 v36, s[12:15], s54 offen lds\n"
    "s_mov_b32 s54, 3538944\n"
    "s_add_u32 s55, s41, 102400\n"
    "s_mov_b32 m0, s55\n"
    "s_nop 0\n"
    "buffer_load_dwordx4 v37, s[12:15], s54 offen lds\n"
    "s_mov_b32 s54, 3670016\n"
    "s_add_u32 s55, s41, 106496\n"
    "s_mov_b32 m0, s55\n"
    "s_nop 0\n"
    "buffer_load_dwordx4 v38, s[12:15], s54 offen lds\n"
    "s_mov_b32 s54, 3801088\n"
    "s_add_u32 s55, s41, 110592\n"
    "s_mov_b32 m0, s55\n"
    "s_nop 0\n"
    "buffer_load_dwordx4 v39, s[12:15], s54 offen lds\n"
    "v_add_u32 v108, 114688, v104\n"
    "v_add_u32 v109, 114688, v105\n"
    "ds_read_b64_tr_b16 a[240:241], v108\n"
    "ds_read_b64_tr_b16 a[242:243], v108 offset:256\n"
    "ds_read_b64_tr_b16 a[244:245], v108 offset:1024\n"
    "ds_read_b64_tr_b16 a[246:247], v108 offset:1280\n"
    "ds_read_b64_tr_b16 v[160:161], v109\n"
    "ds_read_b64_tr_b16 v[162:163], v109 offset:256\n"
    "ds_read_b64_tr_b16 v[164:165], v109 offset:1024\n"
    "ds_read_b64_tr_b16 v[166:167], v109 offset:1280\n"
    "s_waitcnt vmcnt(28)\n"
    "s_waitcnt lgkmcnt(0)\n"
    "s_barrier\n"
    "s_mov_b32 s54, 3932160\n"
    "s_add_u32 s55, s41, 114688\n"
    "s_mov_b32 m0, s55\n"
    "s_nop 0\n"
    "buffer_load_dwordx4 v36, s[12:15], s54 offen lds\n"
    "s_mov_b32 s54, 4063232\n"
    "s_add_u32 s55, s41, 118784\n"
    "s_mov_b32 m0, s55\n"
    "s_nop 0\n"
    "buffer_load_dwordx4 v37, s[12:15], s54 offen lds\n"
    "s_mov_b32 s54, 4194304\n"
    "s_add_u32 s55, s41, 122880\n"
    "s_mov_b32 m0, s55\n"
    "s_nop 0\n"
    "buffer_load_dwordx4 v38, s[12:15], s54 offen lds\n"
    "s_mov_b32 s54, 4325376\n"
    "s_add_u32 s55, s41, 126976\n"
    "s_mov_b32 m0, s55\n"
    "s_nop 0\n"
    "buffer_load_dwordx4 v39, s[12:15], s54 offen lds\n"
    "v_add_u32 v108, 131072, v104\n"
    "v_add_u32 v109, 131072, v105\n"
    "ds_read_b64_tr_b16 a[248:249], v108\n"
    "ds_read_b64_tr_b16 a[250:251], v108 offset:256\n"
    "ds_read_b64_tr_b16 a[252:253], v108 offset:1024\n"
    "ds_read_b64_tr_b16 a[254:255], v108 offset:1280\n"
    "ds_read_b64_tr_b16 v[168:169], v109\n"
    "ds_read_b64_tr_b16 v[170:171], v109 offset:256\n"
    "ds_read_b64_tr_b16 v[172:173], v109 offset:1024\n"
    "ds_read_b64_tr_b16 v[174:175], v109 offset:1280\n"
    "s_waitcnt vmcnt(28)\n"
    "s_waitcnt lgkmcnt(0)\n"
    "s_barrier\n"
    "s_mov_b32 s54, 0\n"
    "s_add_u32 s55, s41, 131072\n"
    "s_mov_b32 m0, s55\n"
    "s_nop 0\n"
    "buffer_load_dwordx4 v40, s[12:15], s54 offen lds\n"
    "s_mov_b32 s54, 131072\n"
    "s_add_u32 s55, s41, 135168\n"
    "s_mov_b32 m0, s55\n"
    "s_nop 0\n"
    "buffer_load_dwordx4 v41, s[12:15], s54 offen lds\n"
    "s_mov_b32 s54, 8192\n"
    "s_add_u32 s55, s41, 139264\n"
    "s_mov_b32 m0, s55\n"
    "s_nop 0\n"
    "buffer_load_dwordx4 v42, s[12:15], s54 offen lds\n"
    "s_mov_b32 s54, 139264\n"
    "s_add_u32 s55, s41, 143360\n"
    "s_mov_b32 m0, s55\n"
    "s_nop 0\n"
    "buffer_load_dwordx4 v43, s[12:15], s54 offen lds\n"
    "v_add_u32 v108, 0, v104\n"
    "v_add_u32 v109, 0, v105\n"
    "ds_read_b64_tr_b16 v[0:1], v108\n"
    "ds_read_b64_tr_b16 v[2:3], v108 offset:256\n"
    "ds_read_b64_tr_b16 v[4:5], v108 offset:1024\n"
    "ds_read_b64_tr_b16 v[6:7], v108 offset:1280\n"
    "ds_read_b64_tr_b16 v[8:9], v109\n"
    "ds_read_b64_tr_b16 v[10:11], v109 offset:256\n"
    "ds_read_b64_tr_b16 v[12:13], v109 offset:1024\n"
    "ds_read_b64_tr_b16 v[14:15], v109 offset:1280\n"
    "s_waitcnt vmcnt(28)\n"
    "s_waitcnt lgkmcnt(0)\n"
    "s_barrier\n"
    "ds_write_b128 v135, v[0:3]\n"
    "ds_write_b128 v135, v[4:7] offset:1024\n"
    "ds_write_b128 v135, v[8:11] offset:2048\n"
    "ds_write_b128 v135, v[12:15] offset:3072\n"
    "v_add_u32 v108, 16384, v104\n"
    "v_add_u32 v109, 16384, v105\n"
    "ds_read_b64_tr_b16 v[16:17], v108\n"
    "ds_read_b64_tr_b16 v[18:19], v108 offset:256\n"
    "ds_read_b64_tr_b16 v[20:21], v108 offset:1024\n"
    "ds_read_b64_tr_b16 v[22:23], v108 offset:1280\n"
    "ds_read_b64_tr_b16 v[24:25], v109\n"
    "ds_read_b64_tr_b16 v[26:27], v109 offset:256\n"
    "ds_read_b64_tr_b16 v[28:29], v109 offset:1024\n"
    "ds_read_b64_tr_b16 v[30:31], v109 offset:1280\n"
    "s_waitcnt vmcnt(24)\n"
    "s_waitcnt lgkmcnt(0)\n"
    "s_barrier\n"
    "ds_write_b128 v135, v[16:19] offset:16384\n"
    "ds_write_b128 v135, v[20:23] offset:17408\n"
    "ds_write_b128 v135, v[24:27] offset:18432\n"
    "ds_write_b128 v135, v[28:31] offset:19456\n"
    "v_add_u32 v108, 32768, v104\n"
    "v_add_u32 v109, 32768, v105\n"
    "ds_read_b64_tr_b16 v[0:1], v108\n"
    "ds_read_b64_tr_b16 v[2:3], v108 offset:256\n"
    "ds_read_b64_tr_b16 v[4:5], v108 offset:1024\n"
    "ds_read_b64_tr_b16 v[6:7], v108 offset:1280\n"
    "ds_read_b64_tr_b16 v[8:9], v109\n"
    "ds_read_b64_tr_b16 v[10:11], v109 offset:256\n"
    "ds_read_b64_tr_b16 v[12:13], v109 offset:1024\n"
    "ds_read_b64_tr_b16 v[14:15], v109 offset:1280\n"
    "s_waitcnt vmcnt(20)\n"
    "s_waitcnt lgkmcnt(0)\n"
    "s_barrier\n"
    "ds_write_b128 v135, v[0:3] offset:32768\n"
    "ds_write_b128 v135, v[4:7] offset:33792\n"
    "ds_write_b128 v135, v[8:11] offset:34816\n"
    "ds_write_b128 v135, v[12:15] offset:35840\n"
    "v_add_u32 v108, 49152, v104\n"
    "v_add_u32 v109, 49152, v105\n"
    "ds_read_b64_tr_b16 v[16:17], v108\n"
    "ds_read_b64_tr_b16 v[18:19], v108 offset:256\n"
    "ds_read_b64_tr_b16 v[20:21], v108 offset:1024\n"
    "ds_read_b64_tr_b16 v[22:23], v108 offset:1280\n"
    "ds_read_b64_tr_b16 v[24:25], v109\n"
    "ds_read_b64_tr_b16 v[26:27], v109 offset:256\n"
    "ds_read_b64_tr_b16 v[28:29], v109 offset:1024\n"
    "ds_read_b64_tr_b16 v[30:31], v109 offset:1280\n"
    "s_waitcnt vmcnt(16)\n"
    "s_waitcnt lgkmcnt(0)\n"
    "s_barrier\n"
    "ds_write_b128 v135, v[16:19] offset:49152\n"
    "ds_write_b128 v135, v[20:23] offset:50176\n"
    "ds_write_b128 v135, v[24:27] offset:51200\n"
    "ds_write_b128 v135, v[28:31] offset:52224\n"
    "v_add_u32 v108, 65536, v104\n"
    "v_add_u32 v109, 65536, v105\n"
    "ds_read_b64_tr_b16 v[0:1], v108\n"
    "ds_read_b64_tr_b16 v[2:3], v108 offset:256\n"
    "ds_read_b64_tr_b16 v[4:5], v108 offset:1024\n"
    "ds_read_b64_tr_b16 v[6:7], v108 offset:1280\n"
    "ds_read_b64_tr_b16 v[8:9], v109\n"
    "ds_read_b64_tr_b16 v[10:11], v109 offset:256\n"
    "ds_read_b64_tr_b16 v[12:13], v109 offset:1024\n"
    "ds_read_b64_tr_b16 v[14:15], v109 offset:1280\n"
    "s_waitcnt vmcnt(12)\n"
    "s_waitcnt lgkmcnt(0)\n"
    "s_barrier\n"
    "ds_write_b128 v136, v[0:3]\n"
    "ds_write_b128 v136, v[4:7] offset:1024\n"
    "ds_write_b128 v136, v[8:11] offset:2048\n"
    "ds_write_b128 v136, v[12:15] offset:3072\n"
    "v_add_u32 v108, 81920, v104\n"
    "v_add_u32 v109, 81920, v105\n"
    "ds_read_b64_tr_b16 v[16:17], v108\n"
    "ds_read_b64_tr_b16 v[18:19], v108 offset:256\n"
    "ds_read_b64_tr_b16 v[20:21], v108 offset:1024\n"
    "ds_read_b64_tr_b16 v[22:23], v108 offset:1280\n"
    "ds_read_b64_tr_b16 v[24:25], v109\n"
    "ds_read_b64_tr_b16 v[26:27], v109 offset:256\n"
    "ds_read_b64_tr_b16 v[28:29], v109 offset:1024\n"
    "ds_read_b64_tr_b16 v[30:31], v109 offset:1280\n"
    "s_waitcnt vmcnt(8)\n"
    "s_waitcnt lgkmcnt(0)\n"
    "s_barrier\n"
    "ds_write_b128 v136, v[16:19] offset:16384\n"
    "ds_write_b128 v136, v[20:23] offset:17408\n"
    "ds_write_b128 v136, v[24:27] offset:18432\n"
    "ds_write_b128 v136, v[28:31] offset:19456\n"
    "v_add_u32 v108, 98304, v104\n"
    "v_add_u32 v109, 98304, v105\n"
    "ds_read_b64_tr_b16 v[0:1], v108\n"
    "ds_read_b64_tr_b16 v[2:3], v108 offset:256\n"
    "ds_read_b64_tr_b16 v[4:5], v108 offset:1024\n"
    "ds_read_b64_tr_b16 v[6:7], v108 offset:1280\n"
    "ds_read_b64_tr_b16 v[8:9], v109\n"
    "ds_read_b64_tr_b16 v[10:11], v109 offset:256\n"
    "ds_read_b64_tr_b16 v[12:13], v109 offset:1024\n"
    "ds_read_b64_tr_b16 v[14:15], v109 offset:1280\n"
    "s_waitcnt vmcnt(4)\n"
    "s_waitcnt lgkmcnt(0)\n"
    "s_barrier\n"
    "ds_write_b128 v136, v[0:3] offset:32768\n"
    "ds_write_b128 v136, v[4:7] offset:33792\n"
    "ds_write_b128 v136, v[8:11] offset:34816\n"
    "ds_write_b128 v136, v[12:15] offset:35840\n"
    "v_add_u32 v108, 114688, v104\n"
    "v_add_u32 v109, 114688, v105\n"
    "ds_read_b64_tr_b16 v[16:17], v108\n"
    "ds_read_b64_tr_b16 v[18:19], v108 offset:256\n"
    "ds_read_b64_tr_b16 v[20:21], v108 offset:1024\n"
    "ds_read_b64_tr_b16 v[22:23], v108 offset:1280\n"
    "ds_read_b64_tr_b16 v[24:25], v109\n"
    "ds_read_b64_tr_b16 v[26:27], v109 offset:256\n"
    "ds_read_b64_tr_b16 v[28:29], v109 offset:1024\n"
    "ds_read_b64_tr_b16 v[30:31], v109 offset:1280\n"
    "s_waitcnt vmcnt(0)\n"
    "s_waitcnt lgkmcnt(0)\n"
    "s_barrier\n"
    "ds_write_b128 v136, v[16:19] offset:49152\n"
    "ds_write_b128 v136, v[20:23] offset:50176\n"
    "ds_write_b128 v136, v[24:27] offset:51200\n"
    "ds_write_b128 v136, v[28:31] offset:52224\n"
    "v_add_u32 v108, 131072, v104\n"
    "v_add_u32 v109, 131072, v105\n"
    "ds_read_b64_tr_b16 v[0:1], v108\n"
    "ds_read_b64_tr_b16 v[2:3], v108 offset:256\n"
    "ds_read_b64_tr_b16 v[4:5], v108 offset:1024\n"
    "ds_read_b64_tr_b16 v[6:7], v108 offset:1280\n"
    "ds_read_b64_tr_b16 v[8:9], v109\n"
    "ds_read_b64_tr_b16 v[10:11], v109 offset:256\n"
    "ds_read_b64_tr_b16 v[12:13], v109 offset:1024\n"
    "ds_read_b64_tr_b16 v[14:15], v109 offset:1280\n"
    "s_waitcnt lgkmcnt(0)\n"
    "s_barrier\n"
    "ds_write_b128 v137, v[0:3]\n"
    "ds_write_b128 v137, v[4:7] offset:1024\n"
    "ds_write_b128 v137, v[8:11] offset:2048\n"
    "ds_write_b128 v137, v[12:15] offset:3072\n"
    "s_mov_b32 s58, 0x00ff00ff\n"
    "s_mov_b32 s59, 0x00ff00ff\n"
    "s_mov_b32 s60, 0\n"
    "s_mov_b32 s61, 0x00ff00ff\n"
    "s_mov_b32 s68, 0\n"
    "s_mov_b32 s69, 0\n"
    "s_not_b64 s[62:63], s[58:59]\n"
    "s_cmp_eq_u32 s26, 0\n"
    "s_cselect_b64 s[66:67], 1, 0\n"
    "s_mov_b32 s56, 0x3e0293ee\n"
    "s_cmp_lt_u32 s26, 2\n"
    "s_cbranch_scc1 XP2\n"
    "v_mov_b32 v240, v112\n"
    "v_mov_b32 v241, v113\n"
    "v_mov_b32 v242, v114\n"
    "v_mov_b32 v243, v115\n"
    "v_mov_b32 v244, v116\n"
    "v_mov_b32 v245, v117\n"
    "v_lshlrev_b32 v246, 0, v121\n"
    "XP2:\n"
    "s_mov_b32 s55, 1507328\n"
    "s_mov_b64 exec, 3\n"
    "buffer_load_dword v0, v121, s[8:11], s55 offen sc1\n"
    "s_waitcnt vmcnt(0)\n"
    "v_readlane_b32 s43, v0, 0\n"
    "v_readlane_b32 s54, v0, 1\n"
    "s_nop 1\n"
    "s_cmp_lt_u32 s54, s43\n"
    "s_cselect_b32 s43, 8192, 0\n"
    "s_mov_b64 exec, 3\n"
    "XP5:\n"
    "buffer_load_dword v0, v121, s[8:11], s33 offen sc1\n"
    "s_waitcnt vmcnt(0)\n"
    "v_cmp_eq_u32 vcc, -1, v0\n"
    "s_nop 0\n"
    "s_cbranch_vccz XP5e\n"
    "s_sleep 2\n"
    "s_add_u32 s53, s53, 1\n"
    "s_cmp_lt_u32 s53, 1048576\n"
    "s_cbranch_scc1 XP5\n"
    "s_branch XFAIL\n"
    "XP5e:\n"
    "s_mov_b64 exec, -1\n"
    "s_add_u32 s52, s32, 16515072\n"
    "buffer_load_dwordx4 v[84:87], v121, s[8:11], s32 offen sc1\n"
    "buffer_load_dwordx4 v[88:91], v121, s[8:11], s32 offen offset:1024 sc1\n"
    "s_mov_b64 s[64:65], 0\n"
    "s_mov_b32 s42, 0\n"
    "s_mov_b32 s14, s44\n"
    "s_mov_b32 s44, s43\n"
    "s_add_u32 s46, s43, s37\n"
    "s_mov_b32 s57, 589824\n"
    "s_mov_b32 s12, 81920\n"
    "s_mov_b32 s13, 163840\n"
    "s_add_u32 s54, s43, 1179648\n"
    "s_add_u32 s96, s54, s41\n"
    "s_add_u32 s98, s54, s38\n"
    "s_lshl_b32 s15, s14, 14\n"
    "s_add_u32 s96, s96, s15\n"
    "s_lshl_b32 s15, s14, 10\n"
    "s_add_u32 s54, s54, s15\n"
    "s_lshr_b32 s55, s41, 1\n"
    "s_add_u32 s97, s54, s55\n"
    "s_add_u32 s97, s97, 65536\n"
    "s_add_u32 s45, s41, 147456\n"
    "s_add_u32 s17, s43, 1540096\n"
    "s_sub_u32 s54, s36, 1508352\n"
    "s_lshr_b32 s54, s54, 3\n"
    "s_add_u32 s16, s17, s54\n"
    "s_cmp_eq_u32 s26, 2\n"
    "s_cselect_b64 s[20:21], 1, 0\n"
    "s_add_u32 s32, s32, 262144\n"
    "s_add_u32 s52, s52, 262144\n"
    "s_and_b32 s54, s42, 3\n"
    "s_cmp_eq_u32 s54, 2\n"
    "s_cselect_b32 s54, 1024, 0\n"
    "s_cmp_lt_u32 s42, 62\n"
    "s_cselect_b32 s54, s54, 0\n"
    "s_add_u32 s33, s33, s54\n"
    "buffer_load_dwordx4 v[92:95], v121, s[8:11], s52 offen sc1\n"
    "buffer_load_dwordx4 v[96:99], v121, s[8:11], s52 offen offset:1024 sc1\n"
    "s_mov_b64 exec, 3\n"
    "buffer_load_dword v134, v121, s[8:11], s33 offen sc1\n"
    "s_mov_b64 exec, -1\n"
    "s_waitcnt lgkmcnt(0)\n"
    "s_barrier\n"
    "s_memrealtime s[24:25]\n"
    "s_mov_b32 s54, 8\n"
    "s_waitcnt lgkmcnt(0)\n"
    "s_mov_b32 s55, s24\n"
    "XE1290:\n"
    "s_memrealtime s[24:25]\n"
    "s_memtime s[14:15]\n"
    "s_sub_u32 s54, s54, 1\n"
    "s_cbranch_scc1 XE1290e\n"
    "s_waitcnt lgkmcnt(0)\n"
    "s_cmp_eq_u32 s24, s55\n"
    "s_cbranch_scc1 XE1290\n"
    "XE1290e:\n"
    "s_waitcnt lgkmcnt(0)\n"
    "s_mul_i32 s24, s24, 24\n"
    "s_sub_u32 s18, s24, s14\n"
    "v_mov_b64 v[36:37], 0\n"
    "v_mov_b64 v[38:39], 0\n"
    "v_mov_b64 v[40:41], 0\n"
    "v_mov_b64 v[42:43], 0\n"
    "v_mov_b64 v[44:45], 0\n"
    "v_mov_b64 v[46:47], 0\n"
    "v_mov_b64 v[48:49], 0\n"
    "v_mov_b64 v[50:51], 0\n"
    "v_mov_b64 v[52:53], 0\n"
    "v_mov_b64 v[54:55], 0\n"
    "v_mov_b64 v[56:57], 0\n"
    "v_mov_b64 v[58:59], 0\n"
    "ds_read_b128 v[60:63], v135\n"
    "ds_read_b128 v[64:67], v135 offset:1024\n"
    "ds_read_b128 v[68:71], v135 offset:16384\n"
    "ds_read_b128 v[72:75], v135 offset:17408\n"
    "ds_read_b128 v[76:79], v135 offset:32768\n"
    "ds_read_b128 v[80:83], v135 offset:33792\n"
    "ds_read_b128 v[104:107], v137\n"
    "ds_read_b128 v[108:111], v137 offset:1024\n"
    "ds_read_b128 v[112:115], v137 offset:2048\n"
    "ds_read_b128 v[116:119], v137 offset:3072\n"
    "s_waitcnt lgkmcnt(0)\n"
    "XTOP:\n"
    "s_waitcnt vmcnt(0)\n"
    "XTOP2:\n"
    "s_memtime s[22:23]\n"
    "s_barrier\n"
    "ds_read_b64_tr_b16 v[0:1], v132\n"
    "ds_read_b64_tr_b16 v[2:3], v132 offset:512\n"
    "ds_read_b64_tr_b16 v[4:5], v132 offset:1024\n"
    "ds_read_b64_tr_b16 v[6:7], v132 offset:1536\n"
    "ds_read_b64_tr_b16 v[8:9], v132 offset:2048\n"
    "ds_read_b64_tr_b16 v[10:11], v132 offset:2560\n"
    "ds_read_b64_tr_b16 v[12:13], v132 offset:3072\n"
    "ds_read_b64_tr_b16 v[14:15], v132 offset:3584\n"
    "s_waitcnt lgkmcnt(6)\n"
    "v_smfmac_f32_16x16x64_bf16 v[36:39], v[0:3], a[0:7], v120\n"
    "ds_read_b64_tr_b16 v[16:17], v132 offset:4096\n"
    "v_smfmac_f32_16x16x64_bf16 v[40:43], v[0:3], a[144:151], v120\n"
    "ds_read_b64_tr_b16 v[18:19], v132 offset:4608\n"
    "s_waitcnt lgkmcnt(6)\n"
    "v_smfmac_f32_16x16x64_bf16 v[36:39], v[4:7], a[8:15], v120\n"
    "ds_read_b64_tr_b16 v[20:21], v132 offset:5120\n"
    "v_smfmac_f32_16x16x64_bf16 v[40:43], v[4:7], a[152:159], v120\n"
    "ds_read_b64_tr_b16 v[22:23], v132 offset:5632\n"
    "s_waitcnt lgkmcnt(6)\n"
    "v_smfmac_f32_16x16x64_bf16 v[36:39], v[8:11], a[16:23], v120\n"
    "ds_read_b64_tr_b16 v[24:25], v132 offset:6144\n"
    "v_smfmac_f32_16x16x64_bf16 v[40:43], v[8:11], a[160:167], v120\n"
    "ds_read_b64_tr_b16 v[26:27], v132 offset:6656\n"
    "s_waitcnt lgkmcnt(6)\n"
    "v_smfmac_f32_16x16x64_bf16 v[36:39], v[12:15], a[24:31], v120\n"
    "ds_read_b64_tr_b16 v[28:29], v132 offset:7168\n"
    "v_smfmac_f32_16x16x64_bf16 v[40:43], v[12:15], a[168:175], v120\n"
    "ds_read_b64_tr_b16 v[30:31], v132 offset:7680\n"
    "s_waitcnt lgkmcnt(6)\n"
    "v_smfmac_f32_16x16x64_bf16 v[36:39], v[16:19], a[32:39], v120\n"
    "ds_read_b64_tr_b16 v[32:33], v132 offset:8192\n"
    "v_smfmac_f32_16x16x64_bf16 v[40:43], v[16:19], a[176:183], v120\n"
    "ds_read_b64_tr_b16 v[34:35], v132 offset:8704\n"
    "s_waitcnt lgkmcnt(6)\n"
    "v_smfmac_f32_16x16x64_bf16 v[36:39], v[20:23], a[40:47], v120\n"
    "v_smfmac_f32_16x16x64_bf16 v[40:43], v[20:23], v[60:67], v120\n"
    "ds_read_b128 v[60:63], v135 offset:49152\n"
    "s_waitcnt lgkmcnt(5)\n"
    "v_smfmac_f32_16x16x64_bf16 v[36:39], v[24:27], a[48:55], v120\n"
    "ds_read_b128 v[64:67], v135 offset:50176\n"
    "v_smfmac_f32_16x16x64_bf16 v[40:43], v[24:27], v[68:75], v120\n"
    "ds_read_b128 v[68:71], v135 offset:2048\n"
    "s_waitcnt lgkmcnt(5)\n"
    "v_smfmac_f32_16x16x64_bf16 v[36:39], v[28:31], a[56:63], v120\n"
    "ds_read_b128 v[72:75], v135 offset:3072\n"
    "v_smfmac_f32_16x16x64_bf16 v[40:43], v[28:31], v[76:83], v120\n"
    "ds_read_b128 v[76:79], v135 offset:18432\n"
    "s_waitcnt lgkmcnt(5)\n"
    "v_smfmac_f32_16x16x64_bf16 v[36:39], v[32:35], a[64:71], v120\n"
    "ds_read_b128 v[80:83], v135 offset:19456\n"
    "s_waitcnt lgkmcnt(4)\n"
    "v_smfmac_f32_16x16x64_bf16 v[40:43], v[32:35], v[60:67], v120\n"
    "ds_read_b128 v[60:63], v135 offset:34816\n"
    "v_smfmac_f32_16x16x64_bf16 v[44:47], v[20:23], a[112:119], v120\n"
    "ds_read_b128 v[64:67], v135 offset:35840\n"
    "s_waitcnt lgkmcnt(4)\n"
    "v_smfmac_f32_16x16x64_bf16 v[48:51], v[20:23], v[68:75], v120\n"
    "ds_read_b128 v[68:71], v135 offset:51200\n"
    "v_smfmac_f32_16x16x64_bf16 v[44:47], v[24:27], a[120:127], v120\n"
    "ds_read_b128 v[72:75], v135 offset:52224\n"
    "v_cvt_pk_bf16_f32 v36, v36, v37\n"
    "s_waitcnt lgkmcnt(4)\n"
    "v_smfmac_f32_16x16x64_bf16 v[48:51], v[24:27], v[76:83], v120\n"
    "ds_read_b128 v[76:79], v136\n"
    "v_smfmac_f32_16x16x64_bf16 v[44:47], v[28:31], a[128:135], v120\n"
    "ds_read_b128 v[80:83], v136 offset:1024\n"
    "v_cvt_pk_bf16_f32 v37, v38, v39\n"
    "s_waitcnt lgkmcnt(4)\n"
    "v_smfmac_f32_16x16x64_bf16 v[48:51], v[28:31], v[60:67], v120\n"
    "ds_read_b128 v[60:63], v136 offset:2048\n"
    "v_smfmac_f32_16x16x64_bf16 v[44:47], v[32:35], a[136:143], v120\n"
    "ds_read_b128 v[64:67], v136 offset:3072\n"
    "v_cvt_pk_bf16_f32 v38, v40, v41\n"
    "s_waitcnt lgkmcnt(4)\n"
    "v_smfmac_f32_16x16x64_bf16 v[48:51], v[32:35], v[68:75], v120\n"
    "ds_read_b128 v[68:71], v136 offset:16384\n"
    "v_smfmac_f32_16x16x64_bf16 v[44:47], v[0:3], a[72:79], v120\n"
    "ds_read_b128 v[72:75], v136 offset:17408\n"
    "v_cvt_pk_bf16_f32 v39, v42, v43\n"
    "v_smfmac_f32_16x16x64_bf16 v[48:51], v[0:3], a[184:191], v120\n"
    "s_nop 0\n"
    "v_mfma_f32_16x16x32_bf16 v[40:43], v[36:39], v[122:125], 0\n"
    "v_smfmac_f32_16x16x64_bf16 v[44:47], v[4:7], a[80:87], v120\n"
    "v_smfmac_f32_16x16x64_bf16 v[48:51], v[4:7], a[192:199], v120\n"
    "s_waitcnt lgkmcnt(0)\n"
    "s_add_u32 s23, s22, s18\n"
    "v_mov_b32 v103, s23\n"
    "v_smfmac_f32_16x16x64_bf16 v[44:47], v[8:11], a[88:95], v120\n"
    "v_cmp_u_f32 vcc, v40, v40\n"
    "v_smfmac_f32_16x16x64_bf16 v[48:51], v[8:11], a[200:207], v120\n"
    "s_cbranch_vccnz XSLOW\n"
    "XCONT:\n"
    "v_cvt_pk_bf16_f32 v100, v40, v41\n"
    "s_mov_b64 exec, s[20:21]\n"
    "buffer_store_dword v103, v121, s[8:11], s16 offen sc1\n"
    "s_mov_b64 exec, -1\n"
    "v_smfmac_f32_16x16x64_bf16 v[44:47], v[12:15], a[96:103], v120\n"
    "v_cvt_pk_bf16_f32 v101, v42, v43\n"
    "s_mov_b32 s99, s12\n"
    "s_mov_b32 s12, s13\n"
    "v_smfmac_f32_16x16x64_bf16 v[48:51], v[12:15], a[208:215], v120\n"
    "v_cndmask_b32 v36, 0, v100, s[58:59]\n"
    "s_add_u32 s13, s13, 81920\n"
    "s_cmp_eq_u32 s13, 327680\n"
    "s_cselect_b32 s13, 0, s13\n"
    "v_smfmac_f32_16x16x64_bf16 v[44:47], v[16:19], a[104:111], v120\n"
    "v_cndmask_b32 v37, 0, v101, s[58:59]\n"
    "s_add_u32 s47, s96, s99\n"
    "s_add_u32 s49, s97, s99\n"
    "v_smfmac_f32_16x16x64_bf16 v[48:51], v[16:19], a[216:223], v120\n"
    "v_mov_b32_dpp v40, v36 row_ror:8 row_mask:0xf bank_mask:0xf\n"
    "s_add_u32 s50, s98, s99\n"
    "s_add_u32 s48, s47, 4096\n"
    "s_add_u32 s51, s98, s13\n"
    "v_smfmac_f32_16x16x64_bf16 v[52:55], v[16:19], v[76:83], v120\n"
    "v_mov_b32_dpp v41, v37 row_ror:8 row_mask:0xf bank_mask:0xf\n"
    "v_cvt_pk_bf16_f32 v44, v44, v45\n"
    "v_cvt_pk_bf16_f32 v45, v46, v47\n"
    "v_cvt_pk_bf16_f32 v46, v48, v49\n"
    "v_cvt_pk_bf16_f32 v47, v50, v51\n"
    "ds_read_b128 v[76:79], v136 offset:18432\n"
    "ds_read_b128 v[80:83], v136 offset:19456\n"
    "v_mfma_f32_16x16x32_bf16 v[48:51], v[44:47], v[122:125], 0\n"
    "v_smfmac_f32_16x16x64_bf16 v[56:59], v[16:19], v[60:67], v120\n"
    "ds_read_b128 v[60:63], v136 offset:32768\n"
    "ds_read_b128 v[64:67], v136 offset:33792\n"
    "s_nop 2\n"
    "v_cvt_pk_bf16_f32 v102, v48, v49\n"
    "v_cvt_pk_bf16_f32 v103, v50, v51\n"
    "v_cndmask_b32 v38, 0, v102, s[58:59]\n"
    "v_cndmask_b32 v39, 0, v103, s[58:59]\n"
    "v_mfma_f32_16x16x32_bf16 v[44:47], v[100:103], v[100:103], 0\n"
    "v_mov_b32_dpp v42, v38 row_ror:8 row_mask:0xf bank_mask:0xf\n"
    "v_mov_b32_dpp v43, v39 row_ror:8 row_mask:0xf bank_mask:0xf\n"
    "v_mfma_f32_16x16x32_bf16 v[48:51], v[84:87], v[36:39], 0\n"
    "s_nop 0\n"
    "v_mfma_f32_16x16x32_bf16 v[48:51], v[88:91], v[40:43], v[48:51]\n"
    "s_mov_b64 exec, s[60:61]\n"
    "s_nop 1\n"
    "ds_write_b128 v138, v[44:47] offset:768\n"
    "s_mov_b64 exec, -1\n"
    "v_smfmac_f32_16x16x64_bf16 v[52:55], v[32:35], v[104:111], v120\n"
    "s_nop 1\n"
    "ds_write_b128 v138, v[48:51]\n"
    "v_smfmac_f32_16x16x64_bf16 v[56:59], v[32:35], v[112:119], v120\n"
    "s_and_b32 s54, s64, 3\n"
    "s_cbranch_scc1 XFS\n"
    "XFO:\n"
    "v_smfmac_f32_16x16x64_bf16 v[52:55], v[0:3], a[224:231], v120\n"
    "buffer_load_dwordx4 v[84:87], v121, s[8:11], s32 offen sc1\n"
    "v_smfmac_f32_16x16x64_bf16 v[56:59], v[0:3], v[144:151], v120\n"
    "buffer_load_dwordx4 v[88:91], v121, s[8:11], s32 offen offset:1024 sc1\n"
    "v_smfmac_f32_16x16x64_bf16 v[52:55], v[4:7], a[232:239], v120\n"
    "v_smfmac_f32_16x16x64_bf16 v[56:59], v[4:7], v[152:159], v120\n"
    "v_smfmac_f32_16x16x64_bf16 v[52:55], v[8:11], a[240:247], v120\n"
    "s_waitcnt lgkmcnt(0)\n"
    "s_barrier\n"
    "ds_read_b128 v[104:107], v139\n"
    "ds_read_b32 v36, v140\n"
    "ds_read_b128 v[108:111], v139 offset:1280\n"
    "ds_read_b32 v37, v140 offset:1280\n"
    "ds_read_b128 v[112:115], v139 offset:2560\n"
    "ds_read_b32 v38, v140 offset:2560\n"
    "ds_read_b128 v[116:119], v139 offset:3840\n"
    "ds_read_b32 v39, v140 offset:3840\n"
    "v_smfmac_f32_16x16x64_bf16 v[52:55], v[20:23], v[68:75], v120\n"
    "ds_read_b128 v[68:71], v136 offset:34816\n"
    "ds_read_b128 v[72:75], v136 offset:35840\n"
    "v_smfmac_f32_16x16x64_bf16 v[56:59], v[20:23], v[76:83], v120\n"
    "ds_read_b128 v[76:79], v136 offset:49152\n"
    "ds_read_b128 v[80:83], v136 offset:50176\n"
    "v_smfmac_f32_16x16x64_bf16 v[52:55], v[24:27], v[60:67], v120\n"
    "ds_read_b128 v[60:63], v136 offset:51200\n"
    "ds_read_b128 v[64:67], v136 offset:52224\n"
    "v_smfmac_f32_16x16x64_bf16 v[56:59], v[8:11], v[160:167], v120\n"
    "s_waitcnt lgkmcnt(11)\n"
    "v_pk_add_f32 v[104:105], v[104:105], v[108:109]\n"
    "v_pk_add_f32 v[106:107], v[106:107], v[110:111]\n"
    "s_waitcnt lgkmcnt(7)\n"
    "v_pk_add_f32 v[112:113], v[112:113], v[116:117]\n"
    "v_pk_add_f32 v[114:115], v[114:115], v[118:119]\n"
    "s_waitcnt lgkmcnt(6)\n"
    "v_pk_add_f32 v[36:37], v[36:37], v[38:39]\n"
    "v_pk_add_f32 v[104:105], v[104:105], v[112:113]\n"
    "v_pk_add_f32 v[106:107], v[106:107], v[114:115]\n"
    "v_add_f32 v108, v36, v37\n"
    "v_pk_mul_f32 v[104:105], v[104:105], s[56:57] op_sel_hi:[1,0]\n"
    "v_pk_mul_f32 v[106:107], v[106:107], s[56:57] op_sel_hi:[1,0]\n"
    "v_mul_f32 v108, s56, v108\n"
    "v_smfmac_f32_16x16x64_bf16 v[52:55], v[12:15], a[248:255], v120\n"
    "v_max3_f32 v109, |v104|, |v105|, |v106|\n"
    "v_exp_f32 v112, v104\n"
    "v_max3_f32 v109, v109, |v107|, |v108|\n"
    "v_smfmac_f32_16x16x64_bf16 v[56:59], v[12:15], v[168:175], v120\n"
    "v_exp_f32 v113, v105\n"
    "v_cmp_lt_f32 vcc, 0x42200000, v109\n"
    "v_exp_f32 v114, v106\n"
    "s_waitcnt lgkmcnt(4)\n"
    "v_smfmac_f32_16x16x64_bf16 v[56:59], v[24:27], v[68:75], v120\n"
    "v_exp_f32 v115, v107\n"
    "v_exp_f32 v116, v108\n"
    "s_cbranch_vccnz XMAX\n"
    "XEXP:\n"
    "v_cvt_pk_bf16_f32 v40, v112, v113\n"
    "v_cvt_pk_bf16_f32 v41, v114, v115\n"
    "v_cvt_pk_bf16_f32 v44, v116, v116\n"
    "v_mov_b32_dpp v42, v40 row_ror:8 row_mask:0xf bank_mask:0xf\n"
    "v_mov_b32_dpp v43, v41 row_ror:8 row_mask:0xf bank_mask:0xf\n"
    "s_waitcnt lgkmcnt(2)\n"
    "v_smfmac_f32_16x16x64_bf16 v[52:55], v[28:31], v[76:83], v120\n"
    "v_mov_b32_dpp v45, v44 row_ror:8 row_mask:0xf bank_mask:0xf\n"
    "s_waitcnt lgkmcnt(0)\n"
    "v_smfmac_f32_16x16x64_bf16 v[56:59], v[28:31], v[60:67], v120\n"
    "v_mfma_f32_16x16x32_bf16 v[48:51], v[126:129], v[40:43], 0\n"
    "v_mov_b64 v[104:105], 0\n"
    "s_waitcnt vmcnt(2)\n"
    "v_mfma_f32_16x16x32_bf16 v[0:3], v[92:95], v[40:43], 0\n"
    "v_cvt_pk_bf16_f32 v16, v52, v53\n"
    "v_mfma_f32_16x16x32_bf16 v[4:7], v[96:99], v[40:43], 0\n"
    "v_cvt_pk_bf16_f32 v17, v54, v55\n"
    "v_cvt_pk_bf16_f32 v18, v56, v57\n"
    "v_mfma_f32_16x16x16_bf16 v[48:51], v[130:131], v[44:45], v[48:51]\n"
    "v_cvt_pk_bf16_f32 v19, v58, v59\n"
    "v_mfma_f32_16x16x16_bf16 v[0:3], v[16:17], v[44:45], v[0:3]\n"
    "v_mov_b64 v[106:107], 0\n"
    "v_mfma_f32_16x16x16_bf16 v[4:7], v[18:19], v[44:45], v[4:7]\n"
    "v_mov_b64 v[108:109], 0\n"
    "v_mov_b64 v[110:111], 0\n"
    "v_mov_b64 v[112:113], 0\n"
    "s_nop 0\n"
    "v_rcp_f32 v48, v48\n"
    "v_mov_b64 v[114:115], 0\n"
    "v_mul_f32 v0, v0, v48\n"
    "v_mul_f32 v1, v1, v48\n"
    "v_mul_f32 v2, v2, v48\n"
    "v_mul_f32 v3, v3, v48\n"
    "v_cvt_pk_bf16_f32 v12, v0, v1\n"
    "v_cvt_pk_bf16_f32 v13, v2, v3\n"
    "v_pk_mul_f32 v[4:5], v[4:5], v[48:49] op_sel_hi:[1,0]\n"
    "v_pk_mul_f32 v[6:7], v[6:7], v[48:49] op_sel_hi:[1,0]\n"
    "v_cvt_pk_bf16_f32 v14, v4, v5\n"
    "v_cvt_pk_bf16_f32 v15, v6, v7\n"
    "s_mov_b64 exec, s[58:59]\n"
    "ds_write_b128 v141, v[12:15]\n"
    "s_mov_b64 exec, -1\n"
    "s_cmp_eq_u32 s42, 63\n"
    "s_cbranch_scc1 XEND\n"
    "v_mov_b64 v[116:117], 0\n"
    "v_mov_b64 v[118:119], 0\n"
    "v_mov_b64 v[36:37], 0\n"
    "v_mov_b64 v[38:39], 0\n"
    "s_cmp_ge_u32 s26, 2\n"
    "s_waitcnt lgkmcnt(0)\n"
    "s_barrier\n"
    "ds_read_b128 v[0:3], v133\n"
    "ds_read_b128 v[4:7], v133 offset:1024\n"
    "s_cbranch_scc1 XB\n"
    "s_waitcnt lgkmcnt(1)\n"
    "v_smfmac_f32_16x16x64_bf16 v[36:39], v[0:3], v[240:247], v120\n"
    "v_smfmac_f32_16x16x64_bf16 v[104:107], v[0:3], v[176:183], v120\n"
    "s_waitcnt lgkmcnt(0)\n"
    "v_smfmac_f32_16x16x64_bf16 v[36:39], v[4:7], v[248:255], v120\n"
    "v_smfmac_f32_16x16x64_bf16 v[104:107], v[4:7], v[184:191], v120\n"
    "v_smfmac_f32_16x16x64_bf16 v[108:111], v[0:3], v[192:199], v120\n"
    "v_smfmac_f32_16x16x64_bf16 v[108:111], v[4:7], v[200:207], v120\n"
    "v_add_f32 v36, v36, v37\n"
    "v_smfmac_f32_16x16x64_bf16 v[112:115], v[0:3], v[208:215], v120\n"
    "v_add_f32 v38, v38, v39\n"
    "v_smfmac_f32_16x16x64_bf16 v[112:115], v[4:7], v[216:223], v120\n"
    "v_cvt_pk_bf16_f32 v12, v36, v38\n"
    "v_smfmac_f32_16x16x64_bf16 v[116:119], v[0:3], v[224:231], v120\n"
    "buffer_store_dword v12, v143, s[8:11], s44 offen sc1\n"
    "v_smfmac_f32_16x16x64_bf16 v[116:119], v[4:7], v[232:239], v120\n"
    "v_add_f32 v104, v104, v105\n"
    "v_add_f32 v106, v106, v107\n"
    "v_cvt_pk_bf16_f32 v8, v104, v106\n"
    "v_add_f32 v108, v108, v109\n"
    "v_add_f32 v110, v110, v111\n"
    "v_cvt_pk_bf16_f32 v9, v108, v110\n"
    "v_add_f32 v112, v112, v113\n"
    "v_add_f32 v114, v114, v115\n"
    "v_cvt_pk_bf16_f32 v10, v112, v114\n"
    "v_add_f32 v116, v116, v117\n"
    "v_add_f32 v118, v118, v119\n"
    "v_cvt_pk_bf16_f32 v11, v116, v118\n"
    "buffer_store_dwordx4 v[8:11], v142, s[8:11], s44 offen sc1\n"
    "s_cmp_eq_u32 s40, 0\n"
    "s_cbranch_scc0 XK1620\n"
    "s_mov_b64 exec, s[62:63]\n"
    "buffer_store_dwordx4 v[100:103], v141, s[8:11], s34 offen sc1\n"
    "s_mov_b64 exec, -1\n"
    "s_branch XK1620e\n"
    "XK1620:\n"
    "buffer_store_dwordx4 v[16:19], v121, s[8:11], s35 offen sc1\n"
    "XK1620e:\n"
    "v_cmp_eq_u32 s[64:65], -1, v134\n"
    "v_mov_b64 v[36:37], 0\n"
    "v_mov_b64 v[38:39], 0\n"
    "v_mov_b64 v[40:41], 0\n"
    "v_mov_b64 v[42:43], 0\n"
    "v_mov_b64 v[44:45], 0\n"
    "v_mov_b64 v[46:47], 0\n"
    "v_mov_b64 v[48:49], 0\n"
    "v_mov_b64 v[50:51], 0\n"
    "v_mov_b64 v[52:53], 0\n"
    "v_mov_b64 v[54:55], 0\n"
    "v_mov_b64 v[56:57], 0\n"
    "v_mov_b64 v[58:59], 0\n"
    "ds_read_b128 v[60:63], v135\n"
    "ds_read_b128 v[64:67], v135 offset:1024\n"
    "ds_read_b128 v[68:71], v135 offset:16384\n"
    "ds_read_b128 v[72:75], v135 offset:17408\n"
    "ds_read_b128 v[76:79], v135 offset:32768\n"
    "ds_read_b128 v[80:83], v135 offset:33792\n"
    "ds_read_b128 v[104:107], v137\n"
    "ds_read_b128 v[108:111], v137 offset:1024\n"
    "ds_read_b128 v[112:115], v137 offset:2048\n"
    "ds_read_b128 v[116:119], v137 offset:3072\n"
    "s_mov_b64 exec, s[66:67]\n"
    "v_mov_b32 v34, s42\n"
    "buffer_store_dword v34, v121, s[8:11], s36 offen sc1\n"
    "s_mov_b64 exec, -1\n"
    "s_waitcnt lgkmcnt(0)\n"
    "s_barrier\n"
    "s_mov_b32 m0, s45\n"
    "s_add_u32 s55, s45, 4096\n"
    "buffer_load_dwordx4 v121, s[8:11], s47 offen sc1 lds\n"
    "s_mov_b32 m0, s55\n"
    "s_lshr_b32 s54, s41, 1\n"
    "buffer_load_dwordx4 v121, s[8:11], s48 offen sc1 lds\n"
    "s_add_u32 m0, s54, 155648\n"
    "s_mov_b32 exec_hi, 0\n"
    "buffer_load_dwordx4 v121, s[8:11], s49 offen sc1 lds\n"
    "s_mov_b64 exec, -1\n"
    "s_add_u32 s42, s42, 1\n"
    "s_add_u32 s44, s44, s57\n"
    "s_add_u32 s46, s46, s57\n"
    "s_sub_u32 s57, 0, s57\n"
    "s_add_u32 s34, s34, 65536\n"
    "s_add_u32 s35, s35, 131072\n"
    "s_and_b32 s54, s42, 7\n"
    "s_cselect_b32 s54, 0, 8192\n"
    "s_add_u32 s54, s54, 1024\n"
    "s_add_u32 s16, s16, s54\n"
    "s_add_u32 s17, s17, s54\n"
    "s_add_u32 s32, s32, 262144\n"
    "s_add_u32 s52, s52, 262144\n"
    "s_and_b32 s54, s42, 3\n"
    "s_cmp_eq_u32 s54, 2\n"
    "s_cselect_b32 s54, 1024, 0\n"
    "s_cmp_lt_u32 s42, 62\n"
    "s_cselect_b32 s54, s54, 0\n"
    "s_add_u32 s33, s33, s54\n"
    "s_mov_b32 s54, s101\n"
    "XD1687:\n"
    "s_sub_u32 s54, s54, 1\n"
    "s_cbranch_scc0 XD1687\n"
    "buffer_load_dwordx4 v[92:95], v121, s[8:11], s52 offen sc1\n"
    "buffer_load_dwordx4 v[96:99], v121, s[8:11], s52 offen offset:1024 sc1\n"
    "s_mov_b64 exec, 3\n"
    "buffer_load_dword v134, v121, s[8:11], s33 offen sc1\n"
    "s_mov_b64 exec, -1\n"
    "s_waitcnt vmcnt(3)\n"
    "s_branch XTOP2\n"
    "XB:\n"
    "buffer_load_dword v32, v246, s[8:11], s17 offen sc1\n"
    "s_waitcnt lgkmcnt(1)\n"
    "v_smfmac_f32_16x16x64_bf16 v[104:107], v[0:3], v[176:183], v120\n"
    "v_smfmac_f32_16x16x64_bf16 v[108:111], v[0:3], v[192:199], v120\n"
    "s_waitcnt lgkmcnt(0)\n"
    "v_smfmac_f32_16x16x64_bf16 v[104:107], v[4:7], v[184:191], v120\n"
    "v_smfmac_f32_16x16x64_bf16 v[108:111], v[4:7], v[200:207], v120\n"
    "v_smfmac_f32_16x16x64_bf16 v[112:115], v[0:3], v[208:215], v120\n"
    "v_smfmac_f32_16x16x64_bf16 v[112:115], v[4:7], v[216:223], v120\n"
    "v_add_f32 v104, v104, v105\n"
    "v_smfmac_f32_16x16x64_bf16 v[116:119], v[0:3], v[224:231], v120\n"
    "v_add_f32 v106, v106, v107\n"
    "v_smfmac_f32_16x16x64_bf16 v[116:119], v[4:7], v[232:239], v120\n"
    "v_cvt_pk_bf16_f32 v8, v104, v106\n"
    "v_add_f32 v108, v108, v109\n"
    "v_add_f32 v110, v110, v111\n"
    "v_cvt_pk_bf16_f32 v9, v108, v110\n"
    "v_add_f32 v112, v112, v113\n"
    "v_add_f32 v114, v114, v115\n"
    "v_cvt_pk_bf16_f32 v10, v112, v114\n"
    "s_nop 0\n"
    "v_add_f32 v116, v116, v117\n"
    "v_add_f32 v118, v118, v119\n"
    "v_cvt_pk_bf16_f32 v11, v116, v118\n"
    "buffer_store_dwordx4 v[8:11], v142, s[8:11], s44 offen sc1\n"
    "s_cmp_lt_i32 s27, 0\n"
    "s_cbranch_scc1 XNOY\n"
    "s_cmp_eq_u32 s40, 0\n"
    "s_cbranch_scc0 XK1726\n"
    "s_mov_b64 exec, s[62:63]\n"
    "buffer_store_dwordx4 v[100:103], v141, s[8:11], s34 offen sc1\n"
    "s_mov_b64 exec, -1\n"
    "s_branch XK1726e\n"
    "XK1726:\n"
    "buffer_store_dwordx4 v[16:19], v121, s[8:11], s35 offen sc1\n"
    "XK1726e:\n"
    "v_cmp_eq_u32 s[64:65], -1, v134\n"
    "v_mov_b64 v[36:37], 0\n"
    "v_mov_b64 v[38:39], 0\n"
    "v_mov_b64 v[40:41], 0\n"
    "v_mov_b64 v[42:43], 0\n"
    "v_mov_b64 v[44:45], 0\n"
    "v_mov_b64 v[46:47], 0\n"
    "v_mov_b64 v[48:49], 0\n"
    "v_mov_b64 v[50:51], 0\n"
    "v_mov_b64 v[52:53], 0\n"
    "v_mov_b64 v[54:55], 0\n"
    "v_mov_b64 v[56:57], 0\n"
    "v_mov_b64 v[58:59], 0\n"
    "ds_read_b128 v[60:63], v135\n"
    "ds_read_b128 v[64:67], v135 offset:1024\n"
    "ds_read_b128 v[68:71], v135 offset:16384\n"
    "ds_read_b128 v[72:75], v135 offset:17408\n"
    "ds_read_b128 v[76:79], v135 offset:32768\n"
    "ds_read_b128 v[80:83], v135 offset:33792\n"
    "ds_read_b128 v[104:107], v137\n"
    "ds_read_b128 v[108:111], v137 offset:1024\n"
    "ds_read_b128 v[112:115], v137 offset:2048\n"
    "ds_read_b128 v[116:119], v137 offset:3072\n"
    "v_mov_b64 v[24:25], -1\n"
    "v_mov_b64 v[26:27], -1\n"
    "s_mov_b32 s68, s46\n"
    "s_waitcnt vmcnt(2)\n"
    "v_cmp_eq_u32 vcc, -1, v32\n"
    "v_subrev_u32 v33, s23, v32\n"
    "s_mov_b32 s55, s100\n"
    "s_cbranch_vccnz XSP\n"
    "XSQ:\n"
    "v_max_i32_dpp v33, v33, v33 row_shr:1 row_mask:0xf bank_mask:0xf\n"
    "s_nop 1\n"
    "v_max_i32_dpp v33, v33, v33 row_shr:2 row_mask:0xf bank_mask:0xf\n"
    "s_nop 1\n"
    "v_max_i32_dpp v33, v33, v33 row_shr:4 row_mask:0xf bank_mask:0xf\n"
    "s_nop 1\n"
    "v_max_i32_dpp v33, v33, v33 row_shr:8 row_mask:0xf bank_mask:0xf\n"
    "s_nop 1\n"
    "v_max_i32_dpp v33, v33, v33 row_bcast:15 row_mask:0xa bank_mask:0xf\n"
    "s_nop 1\n"
    "v_max_i32_dpp v33, v33, v33 row_bcast:31 row_mask:0xc bank_mask:0xf\n"
    "s_nop 0\n"
    "v_readlane_b32 s55, v33, 63\n"
    "s_nop 1\n"
    "s_min_i32 s55, s55, 4000\n"
    "s_add_u32 s55, s55, s22\n"
    "s_add_u32 s19, s55, s28\n"
    "s_memtime s[24:25]\n"
    "s_waitcnt lgkmcnt(0)\n"
    "s_sub_u32 s55, s19, s24\n"
    "s_mul_i32 s55, s55, 73\n"
    "s_ashr_i32 s55, s55, 11\n"
    "s_cmp_lt_i32 s55, 2\n"
    "s_cbranch_scc1 XDYe\n"
    "s_sub_u32 s55, s55, 2\n"
    "XDY:\n"
    "s_sub_u32 s55, s55, 1\n"
    "s_cbranch_scc0 XDY\n"
    "XDYe:\n"
    "s_add_u32 s19, s19, s29\n"
    "XY:\n"
    "buffer_load_dwordx4 v[0:3], v121, s[8:11], s46 offen sc1\n"
    "buffer_load_dwordx4 v[4:7], v121, s[8:11], s46 offen offset:1024 sc1\n"
    "buffer_load_dwordx4 v[8:11], v121, s[8:11], s46 offen offset:2048 sc1\n"
    "buffer_load_dwordx4 v[12:15], v121, s[8:11], s46 offen offset:3072 sc1\n"
    "s_memrealtime s[24:25]\n"
    "s_mov_b32 s54, 8\n"
    "s_waitcnt lgkmcnt(0)\n"
    "s_mov_b32 s55, s24\n"
    "XE1802:\n"
    "s_memrealtime s[24:25]\n"
    "s_memtime s[14:15]\n"
    "s_sub_u32 s54, s54, 1\n"
    "s_cbranch_scc1 XE1802e\n"
    "s_waitcnt lgkmcnt(0)\n"
    "s_cmp_eq_u32 s24, s55\n"
    "s_cbranch_scc1 XE1802\n"
    "XE1802e:\n"
    "s_waitcnt lgkmcnt(0)\n"
    "s_mul_i32 s24, s24, 24\n"
    "s_sub_u32 s18, s24, s14\n"
    "s_waitcnt vmcnt(3)\n"
    "v_mfma_f32_16x16x32_bf16 v[28:31], v[240:243], v[0:3], 0\n"
    "v_max3_u32 v20, v0, v1, v2\n"
    "s_waitcnt vmcnt(2)\n"
    "v_mfma_f32_16x16x32_bf16 v[28:31], v[240:243], v[4:7], v[28:31]\n"
    "v_max3_u32 v20, v20, v3, v4\n"
    "v_max3_u32 v21, v5, v6, v7\n"
    "s_waitcnt vmcnt(1)\n"
    "v_mfma_f32_16x16x32_bf16 v[28:31], v[240:243], v[8:11], v[28:31]\n"
    "v_max3_u32 v20, v20, v8, v9\n"
    "v_max3_u32 v21, v21, v10, v11\n"
    "s_waitcnt vmcnt(0)\n"
    "v_mfma_f32_16x16x32_bf16 v[28:31], v[240:243], v[12:15], v[28:31]\n"
    "v_max3_u32 v20, v20, v12, v13\n"
    "v_max3_u32 v21, v21, v14, v15\n"
    "v_max_u32 v20, v20, v21\n"
    "v_cmp_eq_u32 vcc, -1, v20\n"
    "s_nop 0\n"
    "s_cbranch_vccnz XYR\n"
    "s_nop 1\n"
    "v_add_f32_dpp v28, v28, v28 row_ror:8 row_mask:0xf bank_mask:0xf\n"
    "v_add_f32_dpp v29, v29, v29 row_ror:8 row_mask:0xf bank_mask:0xf\n"
    "v_add_f32_dpp v30, v30, v30 row_ror:8 row_mask:0xf bank_mask:0xf\n"
    "v_add_f32_dpp v31, v31, v31 row_ror:8 row_mask:0xf bank_mask:0xf\n"
    "v_cvt_pk_bf16_f32 v22, v28, v30\n"
    "v_cvt_pk_bf16_f32 v23, v29, v31\n"
    "buffer_store_dwordx2 v[22:23], v244, s[8:11], s50 offen sc1\n"
    "s_memtime s[24:25]\n"
    "s_waitcnt lgkmcnt(0)\n"
    "s_sub_u32 s55, s19, s24\n"
    "s_mul_i32 s55, s55, 73\n"
    "s_ashr_i32 s55, s55, 11\n"
    "s_max_i32 s55, s55, s30\n"
    "s_cmp_lt_i32 s55, 2\n"
    "s_cbranch_scc1 XDZe\n"
    "s_sub_u32 s55, s55, 2\n"
    "XDZ:\n"
    "s_sub_u32 s55, s55, 1\n"
    "s_cbranch_scc0 XDZ\n"
    "XDZe:\n"
    "s_barrier\n"
    "s_mov_b32 m0, s45\n"
    "s_add_u32 s55, s45, 4096\n"
    "buffer_load_dwordx4 v121, s[8:11], s47 offen sc1 lds\n"
    "s_mov_b32 m0, s55\n"
    "s_nop 0\n"
    "buffer_load_dwordx4 v121, s[8:11], s48 offen sc1 lds\n"
    "s_add_u32 s42, s42, 1\n"
    "s_add_u32 s44, s44, s57\n"
    "s_add_u32 s46, s46, s57\n"
    "s_sub_u32 s57, 0, s57\n"
    "s_add_u32 s34, s34, 65536\n"
    "s_add_u32 s35, s35, 131072\n"
    "s_and_b32 s54, s42, 7\n"
    "s_cselect_b32 s54, 0, 8192\n"
    "s_add_u32 s54, s54, 1024\n"
    "s_add_u32 s16, s16, s54\n"
    "s_add_u32 s17, s17, s54\n"
    "s_add_u32 s32, s32, 262144\n"
    "s_add_u32 s52, s52, 262144\n"
    "s_and_b32 s54, s42, 3\n"
    "s_cmp_eq_u32 s54, 2\n"
    "s_cselect_b32 s54, 1024, 0\n"
    "s_cmp_lt_u32 s42, 62\n"
    "s_cselect_b32 s54, s54, 0\n"
    "s_add_u32 s33, s33, s54\n"
    "s_mov_b32 s54, s101\n"
    "XD1884:\n"
    "s_sub_u32 s54, s54, 1\n"
    "s_cbranch_scc0 XD1884\n"
    "buffer_load_dwordx4 v[92:95], v121, s[8:11], s52 offen sc1\n"
    "buffer_load_dwordx4 v[96:99], v121, s[8:11], s52 offen offset:1024 sc1\n"
    "s_mov_b64 exec, 3\n"
    "buffer_load_dword v134, v121, s[8:11], s33 offen sc1\n"
    "s_mov_b64 exec, -1\n"
    "buffer_store_dwordx4 v[24:27], v121, s[8:11], s68 offen sc1\n"
    "buffer_store_dwordx4 v[24:27], v121, s[8:11], s68 offen offset:1024 sc1\n"
    "buffer_store_dwordx4 v[24:27], v121, s[8:11], s68 offen offset:2048 sc1\n"
    "buffer_store_dwordx4 v[24:27], v121, s[8:11], s68 offen offset:3072 sc1\n"
    "s_mov_b32 exec_hi, 0\n"
    "buffer_store_dwordx4 v[24:27], v245, s[8:11], s51 offen sc1\n"
    "s_mov_b64 exec, -1\n"
    "s_waitcnt vmcnt(8)\n"
    "s_branch XTOP2\n"
    "XYR:\n"
    "s_nop 7\n"
    "s_add_u32 s53, s53, 1\n"
    "s_cmp_lt_u32 s53, 1048576\n"
    "s_cbranch_scc1 XY\n"
    "s_branch XFAIL\n"
    "XSP:\n"
    "buffer_load_dword v32, v246, s[8:11], s17 offen sc1\n"
    "s_waitcnt vmcnt(0)\n"
    "v_cmp_eq_u32 vcc, -1, v32\n"
    "v_subrev_u32 v33, s23, v32\n"
    "s_cbranch_vccz XSQ\n"
    "s_sub_u32 s55, s55, 1\n"
    "s_cbranch_scc0 XSP\n"
    "v_mov_b32 v33, 0\n"
    "s_branch XSQ\n"
    "XNOY:\n"
    "s_cmp_eq_u32 s40, 0\n"
    "s_cbranch_scc0 XK1919\n"
    "s_mov_b64 exec, s[62:63]\n"
    "buffer_store_dwordx4 v[100:103], v141, s[8:11], s34 offen sc1\n"
    "s_mov_b64 exec, -1\n"
    "s_branch XK1919e\n"
    "XK1919:\n"
    "buffer_store_dwordx4 v[16:19], v121, s[8:11], s35 offen sc1\n"
    "XK1919e:\n"
    "v_cmp_eq_u32 s[64:65], -1, v134\n"
    "v_mov_b64 v[36:37], 0\n"
    "v_mov_b64 v[38:39], 0\n"
    "v_mov_b64 v[40:41], 0\n"
    "v_mov_b64 v[42:43], 0\n"
    "v_mov_b64 v[44:45], 0\n"
    "v_mov_b64 v[46:47], 0\n"
    "v_mov_b64 v[48:49], 0\n"
    "v_mov_b64 v[50:51], 0\n"
    "v_mov_b64 v[52:53], 0\n"
    "v_mov_b64 v[54:55], 0\n"
    "v_mov_b64 v[56:57], 0\n"
    "v_mov_b64 v[58:59], 0\n"
    "ds_read_b128 v[60:63], v135\n"
    "ds_read_b128 v[64:67], v135 offset:1024\n"
    "ds_read_b128 v[68:71], v135 offset:16384\n"
    "ds_read_b128 v[72:75], v135 offset:17408\n"
    "ds_read_b128 v[76:79], v135 offset:32768\n"
    "ds_read_b128 v[80:83], v135 offset:33792\n"
    "ds_read_b128 v[104:107], v137\n"
    "ds_read_b128 v[108:111], v137 offset:1024\n"
    "ds_read_b128 v[112:115], v137 offset:2048\n"
    "ds_read_b128 v[116:119], v137 offset:3072\n"
    "s_waitcnt lgkmcnt(0)\n"
    "s_barrier\n"
    "s_mov_b32 m0, s45\n"
    "s_add_u32 s55, s45, 4096\n"
    "buffer_load_dwordx4 v121, s[8:11], s47 offen sc1 lds\n"
    "s_mov_b32 m0, s55\n"
    "s_nop 0\n"
    "buffer_load_dwordx4 v121, s[8:11], s48 offen sc1 lds\n"
    "s_add_u32 s42, s42, 1\n"
    "s_add_u32 s44, s44, s57\n"
    "s_add_u32 s46, s46, s57\n"
    "s_sub_u32 s57, 0, s57\n"
    "s_add_u32 s34, s34, 65536\n"
    "s_add_u32 s35, s35, 131072\n"
    "s_and_b32 s54, s42, 7\n"
    "s_cselect_b32 s54, 0, 8192\n"
    "s_add_u32 s54, s54, 1024\n"
    "s_add_u32 s16, s16, s54\n"
    "s_add_u32 s17, s17, s54\n"
    "s_add_u32 s32, s32, 262144\n"
    "s_add_u32 s52, s52, 262144\n"
    "s_and_b32 s54, s42, 3\n"
    "s_cmp_eq_u32 s54, 2\n"
    "s_cselect_b32 s54, 1024, 0\n"
    "s_cmp_lt_u32 s42, 62\n"
    "s_cselect_b32 s54, s54, 0\n"
    "s_add_u32 s33, s33, s54\n"
    "s_mov_b32 s54, s101\n"
    "XD1978:\n"
    "s_sub_u32 s54, s54, 1\n"
    "s_cbranch_scc0 XD1978\n"
    "buffer_load_dwordx4 v[92:95], v121, s[8:11], s52 offen sc1\n"
    "buffer_load_dwordx4 v[96:99], v121, s[8:11], s52 offen offset:1024 sc1\n"
    "s_mov_b64 exec, 3\n"
    "buffer_load_dword v134, v121, s[8:11], s33 offen sc1\n"
    "s_mov_b64 exec, -1\n"
    "s_waitcnt vmcnt(3)\n"
    "s_branch XTOP2\n"
    "XMAX:\n"
    "s_nop 1\n"
    "v_max3_f32 v109, v104, v105, v106\n"
    "v_max3_f32 v109, v109, v107, v108\n"
    "s_nop 1\n"
    "v_max_f32_dpp v109, v109, v109 row_ror:8 row_mask:0xf bank_mask:0xf\n"
    "v_mov_b32 v110, v109\n"
    "s_nop 1\n"
    "v_permlane16_swap_b32 v109, v110\n"
    "v_max_f32 v109, v109, v110\n"
    "v_mov_b32 v110, v109\n"
    "s_nop 1\n"
    "v_permlane32_swap_b32 v109, v110\n"
    "v_max_f32 v109, v109, v110\n"
    "v_sub_f32 v104, v104, v109\n"
    "v_sub_f32 v105, v105, v109\n"
    "v_sub_f32 v106, v106, v109\n"
    "v_sub_f32 v107, v107, v109\n"
    "v_sub_f32 v108, v108, v109\n"
    "v_exp_f32 v112, v104\n"
    "v_exp_f32 v113, v105\n"
    "v_exp_f32 v114, v106\n"
    "v_exp_f32 v115, v107\n"
    "v_exp_f32 v116, v108\n"
    "s_nop 1\n"
    "s_branch XEXP\n"
    "XSLOW:\n"
    "s_cmp_eq_u32 s69, s42\n"
    "s_cbranch_scc1 XCONT\n"
    "s_mov_b32 s69, s42\n"
    "s_waitcnt lgkmcnt(0)\n"
    "s_barrier\n"
    "s_nop 7\n"
    "v_add_u32 v12, s45, v121\n"
    "s_lshr_b32 s54, s41, 1\n"
    "s_add_u32 s54, s54, 155648\n"
    "v_add_u32 v13, s54, v121\n"
    "XS1:\n"
    "s_cmp_ge_u32 s26, 2\n"
    "s_cbranch_scc1 XS2\n"
    "s_mov_b32 m0, s45\n"
    "s_add_u32 s55, s45, 4096\n"
    "buffer_load_dwordx4 v121, s[8:11], s47 offen sc1 lds\n"
    "s_mov_b32 m0, s55\n"
    "s_lshr_b32 s54, s41, 1\n"
    "buffer_load_dwordx4 v121, s[8:11], s48 offen sc1 lds\n"
    "s_add_u32 m0, s54, 155648\n"
    "s_mov_b32 exec_hi, 0\n"
    "buffer_load_dwordx4 v121, s[8:11], s49 offen sc1 lds\n"
    "s_mov_b64 exec, -1\n"
    "s_branch XS3\n"
    "XS2:\n"
    "s_mov_b32 m0, s45\n"
    "s_add_u32 s55, s45, 4096\n"
    "buffer_load_dwordx4 v121, s[8:11], s47 offen sc1 lds\n"
    "s_mov_b32 m0, s55\n"
    "s_nop 0\n"
    "buffer_load_dwordx4 v121, s[8:11], s48 offen sc1 lds\n"
    "XS3:\n"
    "v_mov_b64 v[36:37], 0\n"
    "v_mov_b64 v[38:39], 0\n"
    "v_mov_b64 v[40:41], 0\n"
    "v_mov_b64 v[42:43], 0\n"
    "v_mov_b64 v[44:45], 0\n"
    "v_mov_b64 v[46:47], 0\n"
    "v_mov_b64 v[48:49], 0\n"
    "v_mov_b64 v[50:51], 0\n"
    "ds_read_b128 v[60:63], v135\n"
    "ds_read_b128 v[64:67], v135 offset:1024\n"
    "ds_read_b128 v[68:71], v135 offset:16384\n"
    "ds_read_b128 v[72:75], v135 offset:17408\n"
    "ds_read_b128 v[76:79], v135 offset:32768\n"
    "ds_read_b128 v[80:83], v135 offset:33792\n"
    "v_mov_b64 v[8:9], 0\n"
    "v_mov_b64 v[10:11], 0\n"
    "s_waitcnt vmcnt(0)\n"
    "s_cmp_ge_u32 s26, 2\n"
    "s_cbranch_scc1 XS4\n"
    "s_mov_b32 exec_hi, 0\n"
    "ds_read_b128 v[8:11], v13\n"
    "s_mov_b64 exec, -1\n"
    "XS4:\n"
    "ds_read_b128 v[0:3], v12\n"
    "ds_read_b128 v[4:7], v12 offset:4096\n"
    "s_waitcnt lgkmcnt(0)\n"
    "v_max3_u32 v0, v0, v1, v2\n"
    "v_max3_u32 v0, v0, v3, v4\n"
    "v_max3_u32 v0, v0, v5, v6\n"
    "v_max3_u32 v0, v0, v7, v8\n"
    "v_max3_u32 v0, v0, v9, v10\n"
    "v_max_u32 v0, v0, v11\n"
    "v_cmp_eq_u32 vcc, -1, v0\n"
    "s_nop 0\n"
    "s_cbranch_vccz XTOP\n"
    "s_add_u32 s53, s53, 1\n"
    "s_cmp_lt_u32 s53, 1048576\n"
    "s_cbranch_scc1 XS1\n"
    "s_branch XFAIL\n"
    "XFS:\n"
    "s_mov_b64 exec, 3\n"
    "XF0:\n"
    "buffer_load_dword v36, v121, s[8:11], s33 offen sc1\n"
    "s_waitcnt vmcnt(0)\n"
    "v_cmp_eq_u32 vcc, -1, v36\n"
    "s_nop 0\n"
    "s_cbranch_vccz XF0e\n"
    "s_sleep 2\n"
    "s_add_u32 s53, s53, 1\n"
    "s_cmp_lt_u32 s53, 1048576\n"
    "s_cbranch_scc1 XF0\n"
    "s_branch XFAIL\n"
    "XF0e:\n"
    "s_mov_b64 exec, -1\n"
    "s_branch XFO\n"
    "XEND:\n"
    "s_cmp_eq_u32 s40, 0\n"
    "s_cbranch_scc0 XK2104\n"
    "s_mov_b64 exec, s[62:63]\n"
    "buffer_store_dwordx4 v[100:103], v141, s[8:11], s34 offen sc1\n"
    "s_mov_b64 exec, -1\n"
    "s_branch XK2104e\n"
    "XK2104:\n"
    "buffer_store_dwordx4 v[16:19], v121, s[8:11], s35 offen sc1\n"
    "XK2104e:\n"
    "s_waitcnt vmcnt(0)\n"
    "s_waitcnt lgkmcnt(0)\n"
    "s_barrier\n"
    "s_mov_b64 exec, s[66:67]\n"
    "v_mov_b32 v0, 64\n"
    "buffer_store_dword v0, v121, s[8:11], s36 offen sc1\n"
    "s_endpgm\n"
    "XFAIL:\n"
    "s_mov_b64 exec, 1\n"
    "v_mov_b32 v0, 1\n"
    "s_mov_b32 s54, 1507584\n"
    "s_nop 1\n"
    "buffer_store_dword v0, v121, s[8:11], s54 offen sc1\n"
    "s_endpgm\n"
        :: "{v[120:127]}"(c0), "{v[128:143]}"(c1), "{v[100:107]}"(c2), "{v[112:119]}"(c3), "{s[16:31]}"(sa), "{s[32:47]}"(sb), "{s95}"(so)
        : "memory", "v0", "v255", "a0", "a255", "s8", "s101", "vcc");
}
typedef float f16v __attribute__((ext_vector_type(16)));
DI f16v mf3(v4u a, v4u b, f16v c) { return __builtin_amdgcn_mfma_f32_32x32x16_bf16(BC(b8, a), BC(b8, b), c, 0, 0, 0); }
DI u32 rfl(u32 x) { return __builtin_amdgcn_readfirstlane(x); }
DI float bflo(u32 x) { return BC(float, x << 16); }
DI float bfhi(u32 x) { return BC(float, x & 0xFFFF0000u); }
DI bool wait_ge(rsrc_t rws, u32 vo, u32 off, int want) {
    for (u32 spins = 0;; ++spins) {
        CLOB();
        const int f = (int)bld1(rws, vo, off);
        if (__all(f >= want)) break;
        __builtin_amdgcn_s_sleep(2);
        if (spins >= SPIN_CAP) { bst1(2u, rws, 0, F_ERR); return false; }
    }
    CLOB();
    return true;
}

DI void g_role(const Params& p, const u32 mat, const u32 h) {
    const u32 lane = threadIdx.x & 63, w = rfl(threadIdx.x >> 6);
    const u32 g = lane >> 4, n = lane & 15, q4 = n >> 2, p4 = lane & 3, wr = w >> 1, wc = w & 1;
    const u32 fx = ((n >> 1) & 1) | (q4 << 1), xr = (64 * wr + 8 * q4 + (n & 3)) * 128;
    v16u c0;
    c0[0] = xr + ((g ^ fx) << 4); c0[1] = xr + (((4 + g) ^ fx) << 4);
#pragma unroll
    for (int j = 2; j < 8; ++j) c0[j] = 0;
#pragma unroll
    for (int j = 0; j < 4; ++j)
#pragma unroll
        for (int rho = 0; rho < 2; ++rho) c0[8 + 2 * j + rho] = (8 * g + 4 * rho + q4) * 256 + (((8 * wc + 2 * j + (p4 >> 1)) ^ ((q4 << 2) | ((2 * g + rho) & 3))) << 4) + (p4 & 1) * 8;
    const u32 l16 = lane * 16, xvo = (8 * w + (lane >> 3)) * 1152 + (((lane & 7) ^ (((lane >> 4) & 1) | (w << 1))) << 4), wvo = (4 * w + g) * 8192 + ((n ^ ((g << 2) | w)) << 4);
    const uint64_t a0 = (uint64_t)p.wsa(), a1 = (uint64_t)p.x, a2 = (uint64_t)(mat == 0 ? p.Wq : mat == 1 ? p.Wk : p.Wv) + h * 256;
    v8u rs = {rfl((u32)a0), rfl((u32)(a0 >> 32)), WS_END, 0x20000u, rfl((u32)a1), rfl((u32)(a1 >> 32)), 2048 * 1152, 0x20000u};
    v4u rw = {rfl((u32)a2), rfl((u32)(a2 >> 32)), 576 * 8192, 0x20000u};
    v8u t0 = {w * 1024, rfl((mat == 0 ? WS_Q : mat == 1 ? WS_K : WS_VT) + wr * 2 * 262144 + h * 8192 + wc * 4096), rfl(mat == 0 ? F_TQ + h * 4 : F_TILE + h * 32 + (mat - 1) * 16), rfl(mat == 2),
              128 * 1152, rfl(mat == 0 ? 128 : 1024), 0, 0};
    asm volatile(
    "s_nop 4\n"
    "v_add_u32 v130, 49152, v128\n"
    "v_add_u32 v131, 49152, v129\n"
    "v_add_u32 v132, 98304, v128\n"
    "v_add_u32 v133, 98304, v129\n"
    "s_cmp_eq_u32 s48, 0\n"
    "s_cselect_b64 s[62:63], 1, 0\n"
    "s_mov_b32 s57, 0\n"
    "s_add_u32 m0, s48, 49152\n"
    "s_mov_b32 s60, 0\n"
    "buffer_load_dwordx4 v154, s[44:47], s60 offen lds\n"
    "s_add_u32 m0, s48, 53248\n"
    "s_mov_b32 s60, 131072\n"
    "buffer_load_dwordx4 v154, s[44:47], s60 offen lds\n"
    "s_add_u32 m0, s48, 57344\n"
    "s_mov_b32 s60, 262144\n"
    "buffer_load_dwordx4 v154, s[44:47], s60 offen lds\n"
    "s_add_u32 m0, s48, 61440\n"
    "s_mov_b32 s60, 393216\n"
    "buffer_load_dwordx4 v154, s[44:47], s60 offen lds\n"
    "s_add_u32 m0, s48, 65536\n"
    "s_mov_b32 s60, 524288\n"
    "buffer_load_dwordx4 v154, s[44:47], s60 offen lds\n"
    "s_add_u32 m0, s48, 69632\n"
    "s_mov_b32 s60, 655360\n"
    "buffer_load_dwordx4 v154, s[44:47], s60 offen lds\n"
    "s_add_u32 m0, s48, 73728\n"
    "s_mov_b32 s60, 786432\n"
    "buffer_load_dwordx4 v154, s[44:47], s60 offen lds\n"
    "s_add_u32 m0, s48, 77824\n"
    "s_mov_b32 s60, 917504\n"
    "buffer_load_dwordx4 v154, s[44:47], s60 offen lds\n"
    "s_add_u32 m0, s48, 81920\n"
    "s_mov_b32 s60, 1048576\n"
    "buffer_load_dwordx4 v154, s[44:47], s60 offen lds\n"
    "s_add_u32 m0, s48, 86016\n"
    "s_mov_b32 s60, 1179648\n"
    "buffer_load_dwordx4 v154, s[44:47], s60 offen lds\n"
    "s_add_u32 m0, s48, 90112\n"
    "s_mov_b32 s60, 1310720\n"
    "buffer_load_dwordx4 v154, s[44:47], s60 offen lds\n"
    "s_add_u32 m0, s48, 94208\n"
    "s_mov_b32 s60, 1441792\n"
    "buffer_load_dwordx4 v154, s[44:47], s60 offen lds\n"
    "s_add_u32 m0, s48, 98304\n"
    "s_mov_b32 s60, 1572864\n"
    "buffer_load_dwordx4 v154, s[44:47], s60 offen lds\n"
    "s_add_u32 m0, s48, 102400\n"
    "s_mov_b32 s60, 1703936\n"
    "buffer_load_dwordx4 v154, s[44:47], s60 offen lds\n"
    "s_add_u32 m0, s48, 106496\n"
    "s_mov_b32 s60, 1835008\n"
    "buffer_load_dwordx4 v154, s[44:47], s60 offen lds\n"
    "s_add_u32 m0, s48, 110592\n"
    "s_mov_b32 s60, 1966080\n"
    "buffer_load_dwordx4 v154, s[44:47], s60 offen lds\n"
    "s_add_u32 m0, s48, 114688\n"
    "s_mov_b32 s60, 2097152\n"
    "buffer_load_dwordx4 v154, s[44:47], s60 offen lds\n"
    "s_add_u32 m0, s48, 118784\n"
    "s_mov_b32 s60, 2228224\n"
    "buffer_load_dwordx4 v154, s[44:47], s60 offen lds\n"
    "s_add_u32 m0, s48, 122880\n"
    "s_mov_b32 s60, 2359296\n"
    "buffer_load_dwordx4 v154, s[44:47], s60 offen lds\n"
    "s_add_u32 m0, s48, 126976\n"
    "s_mov_b32 s60, 2490368\n"
    "buffer_load_dwordx4 v154, s[44:47], s60 offen lds\n"
    "s_add_u32 m0, s48, 131072\n"
    "s_mov_b32 s60, 2621440\n"
    "buffer_load_dwordx4 v154, s[44:47], s60 offen lds\n"
    "s_add_u32 m0, s48, 135168\n"
    "s_mov_b32 s60, 2752512\n"
    "buffer_load_dwordx4 v154, s[44:47], s60 offen lds\n"
    "s_add_u32 m0, s48, 139264\n"
    "s_mov_b32 s60, 2883584\n"
    "buffer_load_dwordx4 v154, s[44:47], s60 offen lds\n"
    "s_add_u32 m0, s48, 143360\n"
    "s_mov_b32 s60, 3014656\n"
    "buffer_load_dwordx4 v154, s[44:47], s60 offen lds\n"
    "s_add_u32 m0, s48, 0\n"
    "s_add_u32 s60, s57, 0\n"
    "buffer_load_dwordx4 v153, s[40:43], s60 offen lds\n"
    "s_add_u32 m0, s48, 4096\n"
    "s_add_u32 s60, s57, 36864\n"
    "buffer_load_dwordx4 v153, s[40:43], s60 offen lds\n"
    "s_add_u32 m0, s48, 8192\n"
    "s_add_u32 s60, s57, 73728\n"
    "buffer_load_dwordx4 v153, s[40:43], s60 offen lds\n"
    "s_add_u32 m0, s48, 12288\n"
    "s_add_u32 s60, s57, 110592\n"
    "buffer_load_dwordx4 v153, s[40:43], s60 offen lds\n"
    "s_add_u32 m0, s48, 16384\n"
    "s_add_u32 s60, s57, 128\n"
    "buffer_load_dwordx4 v153, s[40:43], s60 offen lds\n"
    "s_add_u32 m0, s48, 20480\n"
    "s_add_u32 s60, s57, 36992\n"
    "buffer_load_dwordx4 v153, s[40:43], s60 offen lds\n"
    "s_add_u32 m0, s48, 24576\n"
    "s_add_u32 s60, s57, 73856\n"
    "buffer_load_dwordx4 v153, s[40:43], s60 offen lds\n"
    "s_add_u32 m0, s48, 28672\n"
    "s_add_u32 s60, s57, 110720\n"
    "buffer_load_dwordx4 v153, s[40:43], s60 offen lds\n"
    "s_add_u32 m0, s48, 32768\n"
    "s_add_u32 s60, s57, 256\n"
    "buffer_load_dwordx4 v153, s[40:43], s60 offen lds\n"
    "s_add_u32 m0, s48, 36864\n"
    "s_add_u32 s60, s57, 37120\n"
    "buffer_load_dwordx4 v153, s[40:43], s60 offen lds\n"
    "s_add_u32 m0, s48, 40960\n"
    "s_add_u32 s60, s57, 73984\n"
    "buffer_load_dwordx4 v153, s[40:43], s60 offen lds\n"
    "s_add_u32 m0, s48, 45056\n"
    "s_add_u32 s60, s57, 110848\n"
    "buffer_load_dwordx4 v153, s[40:43], s60 offen lds\n"
    "s_waitcnt vmcnt(32)\n"
    "s_waitcnt lgkmcnt(0)\n"
    "s_barrier\n"
    "v_add_u32 v144, 49152, v136\n"
    "v_add_u32 v145, 49152, v137\n"
    "v_add_u32 v146, 49152, v138\n"
    "v_add_u32 v147, 49152, v139\n"
    "v_add_u32 v148, 49152, v140\n"
    "v_add_u32 v149, 49152, v141\n"
    "v_add_u32 v150, 49152, v142\n"
    "v_add_u32 v151, 49152, v143\n"
    "ds_read_b64_tr_b16 a[0:1], v144\n"
    "ds_read_b64_tr_b16 a[2:3], v145\n"
    "ds_read_b64_tr_b16 a[4:5], v146\n"
    "ds_read_b64_tr_b16 a[6:7], v147\n"
    "ds_read_b64_tr_b16 a[8:9], v148\n"
    "ds_read_b64_tr_b16 a[10:11], v149\n"
    "ds_read_b64_tr_b16 a[12:13], v150\n"
    "ds_read_b64_tr_b16 a[14:15], v151\n"
    "ds_read_b64_tr_b16 a[16:17], v144 offset:8192\n"
    "ds_read_b64_tr_b16 a[18:19], v145 offset:8192\n"
    "ds_read_b64_tr_b16 a[20:21], v146 offset:8192\n"
    "ds_read_b64_tr_b16 a[22:23], v147 offset:8192\n"
    "ds_read_b64_tr_b16 a[24:25], v148 offset:8192\n"
    "ds_read_b64_tr_b16 a[26:27], v149 offset:8192\n"
    "ds_read_b64_tr_b16 a[28:29], v150 offset:8192\n"
    "s_waitcnt lgkmcnt(14)\n"
    "ds_read_b64_tr_b16 a[30:31], v151 offset:8192\n"
    "s_waitcnt vmcnt(28)\n"
    "s_waitcnt lgkmcnt(0)\n"
    "s_barrier\n"
    "s_add_u32 m0, s48, 49152\n"
    "s_mov_b32 s60, 3145728\n"
    "buffer_load_dwordx4 v154, s[44:47], s60 offen lds\n"
    "s_add_u32 m0, s48, 53248\n"
    "s_mov_b32 s60, 3276800\n"
    "buffer_load_dwordx4 v154, s[44:47], s60 offen lds\n"
    "s_add_u32 m0, s48, 57344\n"
    "s_mov_b32 s60, 3407872\n"
    "buffer_load_dwordx4 v154, s[44:47], s60 offen lds\n"
    "s_add_u32 m0, s48, 61440\n"
    "s_mov_b32 s60, 3538944\n"
    "buffer_load_dwordx4 v154, s[44:47], s60 offen lds\n"
    "v_add_u32 v144, 65536, v136\n"
    "v_add_u32 v145, 65536, v137\n"
    "v_add_u32 v146, 65536, v138\n"
    "v_add_u32 v147, 65536, v139\n"
    "v_add_u32 v148, 65536, v140\n"
    "v_add_u32 v149, 65536, v141\n"
    "v_add_u32 v150, 65536, v142\n"
    "v_add_u32 v151, 65536, v143\n"
    "ds_read_b64_tr_b16 a[32:33], v144\n"
    "ds_read_b64_tr_b16 a[34:35], v145\n"
    "ds_read_b64_tr_b16 a[36:37], v146\n"
    "ds_read_b64_tr_b16 a[38:39], v147\n"
    "ds_read_b64_tr_b16 a[40:41], v148\n"
    "ds_read_b64_tr_b16 a[42:43], v149\n"
    "ds_read_b64_tr_b16 a[44:45], v150\n"
    "ds_read_b64_tr_b16 a[46:47], v151\n"
    "ds_read_b64_tr_b16 a[48:49], v144 offset:8192\n"
    "ds_read_b64_tr_b16 a[50:51], v145 offset:8192\n"
    "ds_read_b64_tr_b16 a[52:53], v146 offset:8192\n"
    "ds_read_b64_tr_b16 a[54:55], v147 offset:8192\n"
    "ds_read_b64_tr_b16 a[56:57], v148 offset:8192\n"
    "ds_read_b64_tr_b16 a[58:59], v149 offset:8192\n"
    "ds_read_b64_tr_b16 a[60:61], v150 offset:8192\n"
    "s_waitcnt lgkmcnt(14)\n"
    "ds_read_b64_tr_b16 a[62:63], v151 offset:8192\n"
    "s_waitcnt vmcnt(28)\n"
    "s_waitcnt lgkmcnt(0)\n"
    "s_barrier\n"
    "s_add_u32 m0, s48, 65536\n"
    "s_mov_b32 s60, 3670016\n"
    "buffer_load_dwordx4 v154, s[44:47], s60 offen lds\n"
    "s_add_u32 m0, s48, 69632\n"
    "s_mov_b32 s60, 3801088\n"
    "buffer_load_dwordx4 v154, s[44:47], s60 offen lds\n"
    "s_add_u32 m0, s48, 73728\n"
    "s_mov_b32 s60, 3932160\n"
    "buffer_load_dwordx4 v154, s[44:47], s60 offen lds\n"
    "s_add_u32 m0, s48, 77824\n"
    "s_mov_b32 s60, 4063232\n"
    "buffer_load_dwordx4 v154, s[44:47], s60 offen lds\n"
    "v_add_u32 v144, 81920, v136\n"
    "v_add_u32 v145, 81920, v137\n"
    "v_add_u32 v146, 81920, v138\n"
    "v_add_u32 v147, 81920, v139\n"
    "v_add_u32 v148, 81920, v140\n"
    "v_add_u32 v149, 81920, v141\n"
    "v_add_u32 v150, 81920, v142\n"
    "v_add_u32 v151, 81920, v143\n"
    "ds_read_b64_tr_b16 a[64:65], v144\n"
    "ds_read_b64_tr_b16 a[66:67], v145\n"
    "ds_read_b64_tr_b16 a[68:69], v146\n"
    "ds_read_b64_tr_b16 a[70:71], v147\n"
    "ds_read_b64_tr_b16 a[72:73], v148\n"
    "ds_read_b64_tr_b16 a[74:75], v149\n"
    "ds_read_b64_tr_b16 a[76:77], v150\n"
    "ds_read_b64_tr_b16 a[78:79], v151\n"
    "ds_read_b64_tr_b16 a[80:81], v144 offset:8192\n"
    "ds_read_b64_tr_b16 a[82:83], v145 offset:8192\n"
    "ds_read_b64_tr_b16 a[84:85], v146 offset:8192\n"
    "ds_read_b64_tr_b16 a[86:87], v147 offset:8192\n"
    "ds_read_b64_tr_b16 a[88:89], v148 offset:8192\n"
    "ds_read_b64_tr_b16 a[90:91], v149 offset:8192\n"
    "ds_read_b64_tr_b16 a[92:93], v150 offset:8192\n"
    "s_waitcnt lgkmcnt(14)\n"
    "ds_read_b64_tr_b16 a[94:95], v151 offset:8192\n"
    "s_waitcnt vmcnt(28)\n"
    "s_waitcnt lgkmcnt(0)\n"
    "s_barrier\n"
    "s_add_u32 m0, s48, 81920\n"
    "s_mov_b32 s60, 4194304\n"
    "buffer_load_dwordx4 v154, s[44:47], s60 offen lds\n"
    "s_add_u32 m0, s48, 86016\n"
    "s_mov_b32 s60, 4325376\n"
    "buffer_load_dwordx4 v154, s[44:47], s60 offen lds\n"
    "s_add_u32 m0, s48, 90112\n"
    "s_mov_b32 s60, 4456448\n"
    "buffer_load_dwordx4 v154, s[44:47], s60 offen lds\n"
    "s_add_u32 m0, s48, 94208\n"
    "s_mov_b32 s60, 4587520\n"
    "buffer_load_dwordx4 v154, s[44:47], s60 offen lds\n"
    "v_add_u32 v144, 98304, v136\n"
    "v_add_u32 v145, 98304, v137\n"
    "v_add_u32 v146, 98304, v138\n"
    "v_add_u32 v147, 98304, v139\n"
    "v_add_u32 v148, 98304, v140\n"
    "v_add_u32 v149, 98304, v141\n"
    "v_add_u32 v150, 98304, v142\n"
    "v_add_u32 v151, 98304, v143\n"
    "ds_read_b64_tr_b16 a[96:97], v144\n"
    "ds_read_b64_tr_b16 a[98:99], v145\n"
    "ds_read_b64_tr_b16 a[100:101], v146\n"
    "ds_read_b64_tr_b16 a[102:103], v147\n"
    "ds_read_b64_tr_b16 a[104:105], v148\n"
    "ds_read_b64_tr_b16 a[106:107], v149\n"
    "ds_read_b64_tr_b16 a[108:109], v150\n"
    "ds_read_b64_tr_b16 a[110:111], v151\n"
    "ds_read_b64_tr_b16 a[112:113], v144 offset:8192\n"
    "ds_read_b64_tr_b16 a[114:115], v145 offset:8192\n"
    "ds_read_b64_tr_b16 a[116:117], v146 offset:8192\n"
    "ds_read_b64_tr_b16 a[118:119], v147 offset:8192\n"
    "ds_read_b64_tr_b16 a[120:121], v148 offset:8192\n"
    "ds_read_b64_tr_b16 a[122:123], v149 offset:8192\n"
    "ds_read_b64_tr_b16 a[124:125], v150 offset:8192\n"
    "s_waitcnt lgkmcnt(14)\n"
    "ds_read_b64_tr_b16 a[126:127], v151 offset:8192\n"
    "s_waitcnt vmcnt(28)\n"
    "s_waitcnt lgkmcnt(0)\n"
    "s_barrier\n"
    "v_add_u32 v144, 114688, v136\n"
    "v_add_u32 v145, 114688, v137\n"
    "v_add_u32 v146, 114688, v138\n"
    "v_add_u32 v147, 114688, v139\n"
    "v_add_u32 v148, 114688, v140\n"
    "v_add_u32 v149, 114688, v141\n"
    "v_add_u32 v150, 114688, v142\n"
    "v_add_u32 v151, 114688, v143\n"
    "ds_read_b64_tr_b16 a[128:129], v144\n"
    "ds_read_b64_tr_b16 a[130:131], v145\n"
    "ds_read_b64_tr_b16 a[132:133], v146\n"
    "ds_read_b64_tr_b16 a[134:135], v147\n"
    "ds_read_b64_tr_b16 a[136:137], v148\n"
    "ds_read_b64_tr_b16 a[138:139], v149\n"
    "ds_read_b64_tr_b16 a[140:141], v150\n"
    "ds_read_b64_tr_b16 a[142:143], v151\n"
    "ds_read_b64_tr_b16 a[144:145], v144 offset:8192\n"
    "ds_read_b64_tr_b16 a[146:147], v145 offset:8192\n"
    "ds_read_b64_tr_b16 a[148:149], v146 offset:8192\n"
    "ds_read_b64_tr_b16 a[150:151], v147 offset:8192\n"
    "ds_read_b64_tr_b16 a[152:153], v148 offset:8192\n"
    "ds_read_b64_tr_b16 a[154:155], v149 offset:8192\n"
    "ds_read_b64_tr_b16 a[156:157], v150 offset:8192\n"
    "s_waitcnt lgkmcnt(14)\n"
    "ds_read_b64_tr_b16 a[158:159], v151 offset:8192\n"
    "s_waitcnt vmcnt(24)\n"
    "s_waitcnt lgkmcnt(0)\n"
    "s_barrier\n"
    "v_add_u32 v144, 131072, v136\n"
    "v_add_u32 v145, 131072, v137\n"
    "v_add_u32 v146, 131072, v138\n"
    "v_add_u32 v147, 131072, v139\n"
    "v_add_u32 v148, 131072, v140\n"
    "v_add_u32 v149, 131072, v141\n"
    "v_add_u32 v150, 131072, v142\n"
    "v_add_u32 v151, 131072, v143\n"
    "ds_read_b64_tr_b16 a[160:161], v144\n"
    "ds_read_b64_tr_b16 a[162:163], v145\n"
    "ds_read_b64_tr_b16 a[164:165], v146\n"
    "ds_read_b64_tr_b16 a[166:167], v147\n"
    "ds_read_b64_tr_b16 a[168:169], v148\n"
    "ds_read_b64_tr_b16 a[170:171], v149\n"
    "ds_read_b64_tr_b16 a[172:173], v150\n"
    "ds_read_b64_tr_b16 a[174:175], v151\n"
    "ds_read_b64_tr_b16 a[176:177], v144 offset:8192\n"
    "ds_read_b64_tr_b16 a[178:179], v145 offset:8192\n"
    "ds_read_b64_tr_b16 a[180:181], v146 offset:8192\n"
    "ds_read_b64_tr_b16 a[182:183], v147 offset:8192\n"
    "ds_read_b64_tr_b16 a[184:185], v148 offset:8192\n"
    "ds_read_b64_tr_b16 a[186:187], v149 offset:8192\n"
    "ds_read_b64_tr_b16 a[188:189], v150 offset:8192\n"
    "s_waitcnt lgkmcnt(14)\n"
    "ds_read_b64_tr_b16 a[190:191], v151 offset:8192\n"
    "s_waitcnt vmcnt(8)\n"
    "s_waitcnt lgkmcnt(0)\n"
    "s_barrier\n"
    "v_add_u32 v144, 49152, v136\n"
    "v_add_u32 v145, 49152, v137\n"
    "v_add_u32 v146, 49152, v138\n"
    "v_add_u32 v147, 49152, v139\n"
    "v_add_u32 v148, 49152, v140\n"
    "v_add_u32 v149, 49152, v141\n"
    "v_add_u32 v150, 49152, v142\n"
    "v_add_u32 v151, 49152, v143\n"
    "ds_read_b64_tr_b16 a[192:193], v144\n"
    "ds_read_b64_tr_b16 a[194:195], v145\n"
    "ds_read_b64_tr_b16 a[196:197], v146\n"
    "ds_read_b64_tr_b16 a[198:199], v147\n"
    "ds_read_b64_tr_b16 a[200:201], v148\n"
    "ds_read_b64_tr_b16 a[202:203], v149\n"
    "ds_read_b64_tr_b16 a[204:205], v150\n"
    "ds_read_b64_tr_b16 a[206:207], v151\n"
    "ds_read_b64_tr_b16 a[208:209], v144 offset:8192\n"
    "ds_read_b64_tr_b16 a[210:211], v145 offset:8192\n"
    "ds_read_b64_tr_b16 a[212:213], v146 offset:8192\n"
    "ds_read_b64_tr_b16 a[214:215], v147 offset:8192\n"
    "ds_read_b64_tr_b16 a[216:217], v148 offset:8192\n"
    "ds_read_b64_tr_b16 a[218:219], v149 offset:8192\n"
    "ds_read_b64_tr_b16 a[220:221], v150 offset:8192\n"
    "s_waitcnt lgkmcnt(14)\n"
    "ds_read_b64_tr_b16 a[222:223], v151 offset:8192\n"
    "s_waitcnt vmcnt(4)\n"
    "s_waitcnt lgkmcnt(0)\n"
    "s_barrier\n"
    "v_add_u32 v144, 65536, v136\n"
    "v_add_u32 v145, 65536, v137\n"
    "v_add_u32 v146, 65536, v138\n"
    "v_add_u32 v147, 65536, v139\n"
    "v_add_u32 v148, 65536, v140\n"
    "v_add_u32 v149, 65536, v141\n"
    "v_add_u32 v150, 65536, v142\n"
    "v_add_u32 v151, 65536, v143\n"
    "ds_read_b64_tr_b16 a[224:225], v144\n"
    "ds_read_b64_tr_b16 a[226:227], v145\n"
    "ds_read_b64_tr_b16 a[228:229], v146\n"
    "ds_read_b64_tr_b16 a[230:231], v147\n"
    "ds_read_b64_tr_b16 a[232:233], v148\n"
    "ds_read_b64_tr_b16 a[234:235], v149\n"
    "ds_read_b64_tr_b16 a[236:237], v150\n"
    "ds_read_b64_tr_b16 a[238:239], v151\n"
    "ds_read_b64_tr_b16 a[240:241], v144 offset:8192\n"
    "ds_read_b64_tr_b16 a[242:243], v145 offset:8192\n"
    "ds_read_b64_tr_b16 a[244:245], v146 offset:8192\n"
    "ds_read_b64_tr_b16 a[246:247], v147 offset:8192\n"
    "ds_read_b64_tr_b16 a[248:249], v148 offset:8192\n"
    "ds_read_b64_tr_b16 a[250:251], v149 offset:8192\n"
    "ds_read_b64_tr_b16 a[252:253], v150 offset:8192\n"
    "s_waitcnt lgkmcnt(14)\n"
    "ds_read_b64_tr_b16 a[254:255], v151 offset:8192\n"
    "s_waitcnt vmcnt(0)\n"
    "s_waitcnt lgkmcnt(0)\n"
    "s_barrier\n"
    "v_add_u32 v144, 81920, v136\n"
    "v_add_u32 v145, 81920, v137\n"
    "v_add_u32 v146, 81920, v138\n"
    "v_add_u32 v147, 81920, v139\n"
    "v_add_u32 v148, 81920, v140\n"
    "v_add_u32 v149, 81920, v141\n"
    "v_add_u32 v150, 81920, v142\n"
    "v_add_u32 v151, 81920, v143\n"
    "ds_read_b64_tr_b16 v[160:161], v144\n"
    "ds_read_b64_tr_b16 v[162:163], v145\n"
    "ds_read_b64_tr_b16 v[164:165], v146\n"
    "ds_read_b64_tr_b16 v[166:167], v147\n"
    "ds_read_b64_tr_b16 v[168:169], v148\n"
    "ds_read_b64_tr_b16 v[170:171], v149\n"
    "ds_read_b64_tr_b16 v[172:173], v150\n"
    "ds_read_b64_tr_b16 v[174:175], v151\n"
    "ds_read_b64_tr_b16 v[176:177], v144 offset:8192\n"
    "ds_read_b64_tr_b16 v[178:179], v145 offset:8192\n"
    "ds_read_b64_tr_b16 v[180:181], v146 offset:8192\n"
    "ds_read_b64_tr_b16 v[182:183], v147 offset:8192\n"
    "ds_read_b64_tr_b16 v[184:185], v148 offset:8192\n"
    "ds_read_b64_tr_b16 v[186:187], v149 offset:8192\n"
    "ds_read_b64_tr_b16 v[188:189], v150 offset:8192\n"
    "s_waitcnt lgkmcnt(14)\n"
    "ds_read_b64_tr_b16 v[190:191], v151 offset:8192\n"
    "s_waitcnt lgkmcnt(0)\n"
    "s_barrier\n"
    "s_add_u32 m0, s48, 49152\n"
    "s_add_u32 s60, s57, 384\n"
    "buffer_load_dwordx4 v153, s[40:43], s60 offen lds\n"
    "s_add_u32 m0, s48, 53248\n"
    "s_add_u32 s60, s57, 37248\n"
    "buffer_load_dwordx4 v153, s[40:43], s60 offen lds\n"
    "s_add_u32 m0, s48, 57344\n"
    "s_add_u32 s60, s57, 74112\n"
    "buffer_load_dwordx4 v153, s[40:43], s60 offen lds\n"
    "s_add_u32 m0, s48, 61440\n"
    "s_add_u32 s60, s57, 110976\n"
    "buffer_load_dwordx4 v153, s[40:43], s60 offen lds\n"
    "s_add_u32 m0, s48, 65536\n"
    "s_add_u32 s60, s57, 512\n"
    "buffer_load_dwordx4 v153, s[40:43], s60 offen lds\n"
    "s_add_u32 m0, s48, 69632\n"
    "s_add_u32 s60, s57, 37376\n"
    "buffer_load_dwordx4 v153, s[40:43], s60 offen lds\n"
    "s_add_u32 m0, s48, 73728\n"
    "s_add_u32 s60, s57, 74240\n"
    "buffer_load_dwordx4 v153, s[40:43], s60 offen lds\n"
    "s_add_u32 m0, s48, 77824\n"
    "s_add_u32 s60, s57, 111104\n"
    "buffer_load_dwordx4 v153, s[40:43], s60 offen lds\n"
    "s_add_u32 m0, s48, 81920\n"
    "s_add_u32 s60, s57, 640\n"
    "buffer_load_dwordx4 v153, s[40:43], s60 offen lds\n"
    "s_add_u32 m0, s48, 86016\n"
    "s_add_u32 s60, s57, 37504\n"
    "buffer_load_dwordx4 v153, s[40:43], s60 offen lds\n"
    "s_add_u32 m0, s48, 90112\n"
    "s_add_u32 s60, s57, 74368\n"
    "buffer_load_dwordx4 v153, s[40:43], s60 offen lds\n"
    "s_add_u32 m0, s48, 94208\n"
    "s_add_u32 s60, s57, 111232\n"
    "buffer_load_dwordx4 v153, s[40:43], s60 offen lds\n"
    "s_add_u32 m0, s48, 98304\n"
    "s_add_u32 s60, s57, 768\n"
    "buffer_load_dwordx4 v153, s[40:43], s60 offen lds\n"
    "s_add_u32 m0, s48, 102400\n"
    "s_add_u32 s60, s57, 37632\n"
    "buffer_load_dwordx4 v153, s[40:43], s60 offen lds\n"
    "ds_read_b128 v[96:99], v128\n"
    "ds_read_b128 v[100:103], v128 offset:512\n"
    "ds_read_b128 v[104:107], v128 offset:4096\n"
    "ds_read_b128 v[108:111], v128 offset:4608\n"
    "s_mov_b32 s58, 0x70000000\n"
    "s_mov_b32 s59, 0x70000000\n"
    "s_mov_b32 s56, 0\n"
    "s_waitcnt lgkmcnt(0)\n"
    "s_cmp_lg_u32 s51, 0\n"
    "s_cbranch_scc1 GT1\n"
    "GT0:\n"
    "v_mfma_f32_16x16x32_bf16 v[0:3], a[0:3], v[96:99], 0\n"
    "v_mfma_f32_16x16x32_bf16 v[4:7], a[4:7], v[96:99], 0\n"
    "ds_read_b128 v[112:115], v129\n"
    "v_mfma_f32_16x16x32_bf16 v[8:11], a[8:11], v[96:99], 0\n"
    "ds_read_b128 v[116:119], v129 offset:512\n"
    "v_mfma_f32_16x16x32_bf16 v[12:15], a[12:15], v[96:99], 0\n"
    "ds_read_b128 v[120:123], v129 offset:4096\n"
    "v_mfma_f32_16x16x32_bf16 v[16:19], a[0:3], v[100:103], 0\n"
    "ds_read_b128 v[124:127], v129 offset:4608\n"
    "v_mfma_f32_16x16x32_bf16 v[20:23], a[4:7], v[100:103], 0\n"
    "v_mfma_f32_16x16x32_bf16 v[24:27], a[8:11], v[100:103], 0\n"
    "s_add_u32 m0, s48, 106496\n"
    "s_add_u32 s60, s57, 74496\n"
    "buffer_load_dwordx4 v153, s[40:43], s60 offen lds\n"
    "v_mfma_f32_16x16x32_bf16 v[28:31], a[12:15], v[100:103], 0\n"
    "v_mfma_f32_16x16x32_bf16 v[32:35], a[0:3], v[104:107], 0\n"
    "v_mfma_f32_16x16x32_bf16 v[36:39], a[4:7], v[104:107], 0\n"
    "s_add_u32 s61, s58, 0\n"
    "buffer_store_dwordx4 v[64:67], v152, s[36:39], s61 offen sc1\n"
    "v_mfma_f32_16x16x32_bf16 v[40:43], a[8:11], v[104:107], 0\n"
    "v_mfma_f32_16x16x32_bf16 v[44:47], a[12:15], v[104:107], 0\n"
    "v_mfma_f32_16x16x32_bf16 v[48:51], a[0:3], v[108:111], 0\n"
    "s_add_u32 m0, s48, 110592\n"
    "s_add_u32 s60, s57, 111360\n"
    "buffer_load_dwordx4 v153, s[40:43], s60 offen lds\n"
    "v_mfma_f32_16x16x32_bf16 v[52:55], a[4:7], v[108:111], 0\n"
    "v_mfma_f32_16x16x32_bf16 v[56:59], a[8:11], v[108:111], 0\n"
    "buffer_store_dwordx4 v[68:71], v152, s[36:39], s61 offen offset:1024 sc1\n"
    "v_mfma_f32_16x16x32_bf16 v[60:63], a[12:15], v[108:111], 0\n"
    "s_waitcnt lgkmcnt(3)\n"
    "v_mfma_f32_16x16x32_bf16 v[0:3], a[16:19], v[112:115], v[0:3]\n"
    "v_mfma_f32_16x16x32_bf16 v[4:7], a[20:23], v[112:115], v[4:7]\n"
    "ds_read_b128 v[96:99], v128 offset:16384\n"
    "v_mfma_f32_16x16x32_bf16 v[8:11], a[24:27], v[112:115], v[8:11]\n"
    "ds_read_b128 v[100:103], v128 offset:16896\n"
    "v_mfma_f32_16x16x32_bf16 v[12:15], a[28:31], v[112:115], v[12:15]\n"
    "ds_read_b128 v[104:107], v128 offset:20480\n"
    "s_waitcnt lgkmcnt(5)\n"
    "v_mfma_f32_16x16x32_bf16 v[16:19], a[16:19], v[116:119], v[16:19]\n"
    "ds_read_b128 v[108:111], v128 offset:20992\n"
    "v_mfma_f32_16x16x32_bf16 v[20:23], a[20:23], v[116:119], v[20:23]\n"
    "v_mfma_f32_16x16x32_bf16 v[24:27], a[24:27], v[116:119], v[24:27]\n"
    "s_add_u32 m0, s48, 114688\n"
    "s_add_u32 s60, s57, 896\n"
    "buffer_load_dwordx4 v153, s[40:43], s60 offen lds\n"
    "v_mfma_f32_16x16x32_bf16 v[28:31], a[28:31], v[116:119], v[28:31]\n"
    "s_waitcnt lgkmcnt(5)\n"
    "v_mfma_f32_16x16x32_bf16 v[32:35], a[16:19], v[120:123], v[32:35]\n"
    "v_mfma_f32_16x16x32_bf16 v[36:39], a[20:23], v[120:123], v[36:39]\n"
    "buffer_store_dwordx4 v[72:75], v152, s[36:39], s61 offen offset:2048 sc1\n"
    "v_mfma_f32_16x16x32_bf16 v[40:43], a[24:27], v[120:123], v[40:43]\n"
    "v_mfma_f32_16x16x32_bf16 v[44:47], a[28:31], v[120:123], v[44:47]\n"
    "s_waitcnt lgkmcnt(4)\n"
    "v_mfma_f32_16x16x32_bf16 v[48:51], a[16:19], v[124:127], v[48:51]\n"
    "s_add_u32 m0, s48, 118784\n"
    "s_add_u32 s60, s57, 37760\n"
    "buffer_load_dwordx4 v153, s[40:43], s60 offen lds\n"
    "v_mfma_f32_16x16x32_bf16 v[52:55], a[20:23], v[124:127], v[52:55]\n"
    "v_mfma_f32_16x16x32_bf16 v[56:59], a[24:27], v[124:127], v[56:59]\n"
    "buffer_store_dwordx4 v[76:79], v152, s[36:39], s61 offen offset:3072 sc1\n"
    "v_mfma_f32_16x16x32_bf16 v[60:63], a[28:31], v[124:127], v[60:63]\n"
    "s_waitcnt lgkmcnt(3)\n"
    "v_mfma_f32_16x16x32_bf16 v[0:3], a[32:35], v[96:99], v[0:3]\n"
    "v_mfma_f32_16x16x32_bf16 v[4:7], a[36:39], v[96:99], v[4:7]\n"
    "ds_read_b128 v[112:115], v129 offset:16384\n"
    "v_mfma_f32_16x16x32_bf16 v[8:11], a[40:43], v[96:99], v[8:11]\n"
    "ds_read_b128 v[116:119], v129 offset:16896\n"
    "v_mfma_f32_16x16x32_bf16 v[12:15], a[44:47], v[96:99], v[12:15]\n"
    "ds_read_b128 v[120:123], v129 offset:20480\n"
    "s_waitcnt lgkmcnt(5)\n"
    "v_mfma_f32_16x16x32_bf16 v[16:19], a[32:35], v[100:103], v[16:19]\n"
    "ds_read_b128 v[124:127], v129 offset:20992\n"
    "v_mfma_f32_16x16x32_bf16 v[20:23], a[36:39], v[100:103], v[20:23]\n"
    "v_mfma_f32_16x16x32_bf16 v[24:27], a[40:43], v[100:103], v[24:27]\n"
    "s_add_u32 m0, s48, 122880\n"
    "s_add_u32 s60, s57, 74624\n"
    "buffer_load_dwordx4 v153, s[40:43], s60 offen lds\n"
    "v_mfma_f32_16x16x32_bf16 v[28:31], a[44:47], v[100:103], v[28:31]\n"
    "s_waitcnt lgkmcnt(5)\n"
    "v_mfma_f32_16x16x32_bf16 v[32:35], a[32:35], v[104:107], v[32:35]\n"
    "v_mfma_f32_16x16x32_bf16 v[36:39], a[36:39], v[104:107], v[36:39]\n"
    "s_add_u32 s61, s58, 262144\n"
    "buffer_store_dwordx4 v[80:83], v152, s[36:39], s61 offen sc1\n"
    "v_mfma_f32_16x16x32_bf16 v[40:43], a[40:43], v[104:107], v[40:43]\n"
    "v_mfma_f32_16x16x32_bf16 v[44:47], a[44:47], v[104:107], v[44:47]\n"
    "s_waitcnt lgkmcnt(4)\n"
    "v_mfma_f32_16x16x32_bf16 v[48:51], a[32:35], v[108:111], v[48:51]\n"
    "s_add_u32 m0, s48, 126976\n"
    "s_add_u32 s60, s57, 111488\n"
    "buffer_load_dwordx4 v153, s[40:43], s60 offen lds\n"
    "v_mfma_f32_16x16x32_bf16 v[52:55], a[36:39], v[108:111], v[52:55]\n"
    "v_mfma_f32_16x16x32_bf16 v[56:59], a[40:43], v[108:111], v[56:59]\n"
    "buffer_store_dwordx4 v[84:87], v152, s[36:39], s61 offen offset:1024 sc1\n"
    "v_mfma_f32_16x16x32_bf16 v[60:63], a[44:47], v[108:111], v[60:63]\n"
    "s_waitcnt lgkmcnt(3)\n"
    "v_mfma_f32_16x16x32_bf16 v[0:3], a[48:51], v[112:115], v[0:3]\n"
    "v_mfma_f32_16x16x32_bf16 v[4:7], a[52:55], v[112:115], v[4:7]\n"
    "ds_read_b128 v[96:99], v128 offset:32768\n"
    "v_mfma_f32_16x16x32_bf16 v[8:11], a[56:59], v[112:115], v[8:11]\n"
    "ds_read_b128 v[100:103], v128 offset:33280\n"
    "v_mfma_f32_16x16x32_bf16 v[12:15], a[60:63], v[112:115], v[12:15]\n"
    "ds_read_b128 v[104:107], v128 offset:36864\n"
    "s_waitcnt lgkmcnt(5)\n"
    "v_mfma_f32_16x16x32_bf16 v[16:19], a[48:51], v[116:119], v[16:19]\n"
    "ds_read_b128 v[108:111], v128 offset:37376\n"
    "v_mfma_f32_16x16x32_bf16 v[20:23], a[52:55], v[116:119], v[20:23]\n"
    "v_mfma_f32_16x16x32_bf16 v[24:27], a[56:59], v[116:119], v[24:27]\n"
    "s_add_u32 m0, s48, 131072\n"
    "s_add_u32 s60, s57, 1024\n"
    "buffer_load_dwordx4 v153, s[40:43], s60 offen lds\n"
    "v_mfma_f32_16x16x32_bf16 v[28:31], a[60:63], v[116:119], v[28:31]\n"
    "s_waitcnt lgkmcnt(5)\n"
    "v_mfma_f32_16x16x32_bf16 v[32:35], a[48:51], v[120:123], v[32:35]\n"
    "v_mfma_f32_16x16x32_bf16 v[36:39], a[52:55], v[120:123], v[36:39]\n"
    "buffer_store_dwordx4 v[88:91], v152, s[36:39], s61 offen offset:2048 sc1\n"
    "v_mfma_f32_16x16x32_bf16 v[40:43], a[56:59], v[120:123], v[40:43]\n"
    "v_mfma_f32_16x16x32_bf16 v[44:47], a[60:63], v[120:123], v[44:47]\n"
    "s_waitcnt lgkmcnt(4)\n"
    "v_mfma_f32_16x16x32_bf16 v[48:51], a[48:51], v[124:127], v[48:51]\n"
    "s_add_u32 m0, s48, 135168\n"
    "s_add_u32 s60, s57, 37888\n"
    "buffer_load_dwordx4 v153, s[40:43], s60 offen lds\n"
    "v_mfma_f32_16x16x32_bf16 v[52:55], a[52:55], v[124:127], v[52:55]\n"
    "v_mfma_f32_16x16x32_bf16 v[56:59], a[56:59], v[124:127], v[56:59]\n"
    "buffer_store_dwordx4 v[92:95], v152, s[36:39], s61 offen offset:3072 sc1\n"
    "v_mfma_f32_16x16x32_bf16 v[60:63], a[60:63], v[124:127], v[60:63]\n"
    "s_waitcnt lgkmcnt(3)\n"
    "v_mfma_f32_16x16x32_bf16 v[0:3], a[64:67], v[96:99], v[0:3]\n"
    "v_mfma_f32_16x16x32_bf16 v[4:7], a[68:71], v[96:99], v[4:7]\n"
    "ds_read_b128 v[112:115], v129 offset:32768\n"
    "v_mfma_f32_16x16x32_bf16 v[8:11], a[72:75], v[96:99], v[8:11]\n"
    "ds_read_b128 v[116:119], v129 offset:33280\n"
    "v_mfma_f32_16x16x32_bf16 v[12:15], a[76:79], v[96:99], v[12:15]\n"
    "ds_read_b128 v[120:123], v129 offset:36864\n"
    "s_waitcnt lgkmcnt(5)\n"
    "v_mfma_f32_16x16x32_bf16 v[16:19], a[64:67], v[100:103], v[16:19]\n"
    "ds_read_b128 v[124:127], v129 offset:37376\n"
    "v_mfma_f32_16x16x32_bf16 v[20:23], a[68:71], v[100:103], v[20:23]\n"
    "v_mfma_f32_16x16x32_bf16 v[24:27], a[72:75], v[100:103], v[24:27]\n"
    "s_add_u32 m0, s48, 139264\n"
    "s_add_u32 s60, s57, 74752\n"
    "buffer_load_dwordx4 v153, s[40:43], s60 offen lds\n"
    "v_mfma_f32_16x16x32_bf16 v[28:31], a[76:79], v[100:103], v[28:31]\n"
    "s_waitcnt lgkmcnt(5)\n"
    "v_mfma_f32_16x16x32_bf16 v[32:35], a[64:67], v[104:107], v[32:35]\n"
    "v_mfma_f32_16x16x32_bf16 v[36:39], a[68:71], v[104:107], v[36:39]\n"
    "v_mfma_f32_16x16x32_bf16 v[40:43], a[72:75], v[104:107], v[40:43]\n"
    "v_mfma_f32_16x16x32_bf16 v[44:47], a[76:79], v[104:107], v[44:47]\n"
    "s_waitcnt lgkmcnt(4)\n"
    "v_mfma_f32_16x16x32_bf16 v[48:51], a[64:67], v[108:111], v[48:51]\n"
    "s_add_u32 m0, s48, 143360\n"
    "s_add_u32 s60, s57, 111616\n"
    "buffer_load_dwordx4 v153, s[40:43], s60 offen lds\n"
    "v_mfma_f32_16x16x32_bf16 v[52:55], a[68:71], v[108:111], v[52:55]\n"
    "v_mfma_f32_16x16x32_bf16 v[56:59], a[72:75], v[108:111], v[56:59]\n"
    "v_mfma_f32_16x16x32_bf16 v[60:63], a[76:79], v[108:111], v[60:63]\n"
    "s_waitcnt lgkmcnt(0)\n"
    "s_waitcnt vmcnt(20)\n"
    "s_barrier\n"
    "s_mov_b32 s57, s52\n"
    "v_mfma_f32_16x16x32_bf16 v[0:3], a[80:83], v[112:115], v[0:3]\n"
    "v_mfma_f32_16x16x32_bf16 v[4:7], a[84:87], v[112:115], v[4:7]\n"
    "ds_read_b128 v[96:99], v130\n"
    "v_mfma_f32_16x16x32_bf16 v[8:11], a[88:91], v[112:115], v[8:11]\n"
    "ds_read_b128 v[100:103], v130 offset:512\n"
    "v_mfma_f32_16x16x32_bf16 v[12:15], a[92:95], v[112:115], v[12:15]\n"
    "ds_read_b128 v[104:107], v130 offset:4096\n"
    "v_mfma_f32_16x16x32_bf16 v[16:19], a[80:83], v[116:119], v[16:19]\n"
    "ds_read_b128 v[108:111], v130 offset:4608\n"
    "v_mfma_f32_16x16x32_bf16 v[20:23], a[84:87], v[116:119], v[20:23]\n"
    "v_mfma_f32_16x16x32_bf16 v[24:27], a[88:91], v[116:119], v[24:27]\n"
    "s_add_u32 m0, s48, 0\n"
    "s_add_u32 s60, s57, 0\n"
    "buffer_load_dwordx4 v153, s[40:43], s60 offen lds\n"
    "v_mfma_f32_16x16x32_bf16 v[28:31], a[92:95], v[116:119], v[28:31]\n"
    "v_mfma_f32_16x16x32_bf16 v[32:35], a[80:83], v[120:123], v[32:35]\n"
    "v_mfma_f32_16x16x32_bf16 v[36:39], a[84:87], v[120:123], v[36:39]\n"
    "v_mfma_f32_16x16x32_bf16 v[40:43], a[88:91], v[120:123], v[40:43]\n"
    "v_mfma_f32_16x16x32_bf16 v[44:47], a[92:95], v[120:123], v[44:47]\n"
    "v_mfma_f32_16x16x32_bf16 v[48:51], a[80:83], v[124:127], v[48:51]\n"
    "s_add_u32 m0, s48, 4096\n"
    "s_add_u32 s60, s57, 36864\n"
    "buffer_load_dwordx4 v153, s[40:43], s60 offen lds\n"
    "v_mfma_f32_16x16x32_bf16 v[52:55], a[84:87], v[124:127], v[52:55]\n"
    "v_mfma_f32_16x16x32_bf16 v[56:59], a[88:91], v[124:127], v[56:59]\n"
    "v_mfma_f32_16x16x32_bf16 v[60:63], a[92:95], v[124:127], v[60:63]\n"
    "s_waitcnt lgkmcnt(3)\n"
    "v_mfma_f32_16x16x32_bf16 v[0:3], a[96:99], v[96:99], v[0:3]\n"
    "v_mfma_f32_16x16x32_bf16 v[4:7], a[100:103], v[96:99], v[4:7]\n"
    "ds_read_b128 v[112:115], v131\n"
    "v_mfma_f32_16x16x32_bf16 v[8:11], a[104:107], v[96:99], v[8:11]\n"
    "ds_read_b128 v[116:119], v131 offset:512\n"
    "v_mfma_f32_16x16x32_bf16 v[12:15], a[108:111], v[96:99], v[12:15]\n"
    "ds_read_b128 v[120:123], v131 offset:4096\n"
    "s_waitcnt lgkmcnt(5)\n"
    "v_mfma_f32_16x16x32_bf16 v[16:19], a[96:99], v[100:103], v[16:19]\n"
    "ds_read_b128 v[124:127], v131 offset:4608\n"
    "v_mfma_f32_16x16x32_bf16 v[20:23], a[100:103], v[100:103], v[20:23]\n"
    "v_mfma_f32_16x16x32_bf16 v[24:27], a[104:107], v[100:103], v[24:27]\n"
    "s_add_u32 m0, s48, 8192\n"
    "s_add_u32 s60, s57, 73728\n"
    "buffer_load_dwordx4 v153, s[40:43], s60 offen lds\n"
    "v_mfma_f32_16x16x32_bf16 v[28:31], a[108:111], v[100:103], v[28:31]\n"
    "s_waitcnt lgkmcnt(5)\n"
    "v_mfma_f32_16x16x32_bf16 v[32:35], a[96:99], v[104:107], v[32:35]\n"
    "v_mfma_f32_16x16x32_bf16 v[36:39], a[100:103], v[104:107], v[36:39]\n"
    "v_mfma_f32_16x16x32_bf16 v[40:43], a[104:107], v[104:107], v[40:43]\n"
    "v_mfma_f32_16x16x32_bf16 v[44:47], a[108:111], v[104:107], v[44:47]\n"
    "s_waitcnt lgkmcnt(4)\n"
    "v_mfma_f32_16x16x32_bf16 v[48:51], a[96:99], v[108:111], v[48:51]\n"
    "s_add_u32 m0, s48, 12288\n"
    "s_add_u32 s60, s57, 110592\n"
    "buffer_load_dwordx4 v153, s[40:43], s60 offen lds\n"
    "v_mfma_f32_16x16x32_bf16 v[52:55], a[100:103], v[108:111], v[52:55]\n"
    "v_mfma_f32_16x16x32_bf16 v[56:59], a[104:107], v[108:111], v[56:59]\n"
    "v_mfma_f32_16x16x32_bf16 v[60:63], a[108:111], v[108:111], v[60:63]\n"
    "s_waitcnt lgkmcnt(3)\n"
    "v_mfma_f32_16x16x32_bf16 v[0:3], a[112:115], v[112:115], v[0:3]\n"
    "v_mfma_f32_16x16x32_bf16 v[4:7], a[116:119], v[112:115], v[4:7]\n"
    "ds_read_b128 v[96:99], v130 offset:16384\n"
    "v_mfma_f32_16x16x32_bf16 v[8:11], a[120:123], v[112:115], v[8:11]\n"
    "ds_read_b128 v[100:103], v130 offset:16896\n"
    "v_mfma_f32_16x16x32_bf16 v[12:15], a[124:127], v[112:115], v[12:15]\n"
    "ds_read_b128 v[104:107], v130 offset:20480\n"
    "s_waitcnt lgkmcnt(5)\n"
    "v_mfma_f32_16x16x32_bf16 v[16:19], a[112:115], v[116:119], v[16:19]\n"
    "ds_read_b128 v[108:111], v130 offset:20992\n"
    "v_mfma_f32_16x16x32_bf16 v[20:23], a[116:119], v[116:119], v[20:23]\n"
    "v_mfma_f32_16x16x32_bf16 v[24:27], a[120:123], v[116:119], v[24:27]\n"
    "s_add_u32 m0, s48, 16384\n"
    "s_add_u32 s60, s57, 128\n"
    "buffer_load_dwordx4 v153, s[40:43], s60 offen lds\n"
    "v_mfma_f32_16x16x32_bf16 v[28:31], a[124:127], v[116:119], v[28:31]\n"
    "s_waitcnt lgkmcnt(5)\n"
    "v_mfma_f32_16x16x32_bf16 v[32:35], a[112:115], v[120:123], v[32:35]\n"
    "v_mfma_f32_16x16x32_bf16 v[36:39], a[116:119], v[120:123], v[36:39]\n"
    "v_mfma_f32_16x16x32_bf16 v[40:43], a[120:123], v[120:123], v[40:43]\n"
    "v_mfma_f32_16x16x32_bf16 v[44:47], a[124:127], v[120:123], v[44:47]\n"
    "s_waitcnt lgkmcnt(4)\n"
    "v_mfma_f32_16x16x32_bf16 v[48:51], a[112:115], v[124:127], v[48:51]\n"
    "s_add_u32 m0, s48, 20480\n"
    "s_add_u32 s60, s57, 36992\n"
    "buffer_load_dwordx4 v153, s[40:43], s60 offen lds\n"
    "v_mfma_f32_16x16x32_bf16 v[52:55], a[116:119], v[124:127], v[52:55]\n"
    "v_mfma_f32_16x16x32_bf16 v[56:59], a[120:123], v[124:127], v[56:59]\n"
    "v_mfma_f32_16x16x32_bf16 v[60:63], a[124:127], v[124:127], v[60:63]\n"
    "s_waitcnt lgkmcnt(3)\n"
    "v_mfma_f32_16x16x32_bf16 v[0:3], a[128:131], v[96:99], v[0:3]\n"
    "v_mfma_f32_16x16x32_bf16 v[4:7], a[132:135], v[96:99], v[4:7]\n"
    "ds_read_b128 v[112:115], v131 offset:16384\n"
    "v_mfma_f32_16x16x32_bf16 v[8:11], a[136:139], v[96:99], v[8:11]\n"
    "ds_read_b128 v[116:119], v131 offset:16896\n"
    "v_mfma_f32_16x16x32_bf16 v[12:15], a[140:143], v[96:99], v[12:15]\n"
    "ds_read_b128 v[120:123], v131 offset:20480\n"
    "s_waitcnt lgkmcnt(5)\n"
    "v_mfma_f32_16x16x32_bf16 v[16:19], a[128:131], v[100:103], v[16:19]\n"
    "ds_read_b128 v[124:127], v131 offset:20992\n"
    "v_mfma_f32_16x16x32_bf16 v[20:23], a[132:135], v[100:103], v[20:23]\n"
    "v_mfma_f32_16x16x32_bf16 v[24:27], a[136:139], v[100:103], v[24:27]\n"
    "s_add_u32 m0, s48, 24576\n"
    "s_add_u32 s60, s57, 73856\n"
    "buffer_load_dwordx4 v153, s[40:43], s60 offen lds\n"
    "v_mfma_f32_16x16x32_bf16 v[28:31], a[140:143], v[100:103], v[28:31]\n"
    "s_waitcnt lgkmcnt(5)\n"
    "v_mfma_f32_16x16x32_bf16 v[32:35], a[128:131], v[104:107], v[32:35]\n"
    "v_mfma_f32_16x16x32_bf16 v[36:39], a[132:135], v[104:107], v[36:39]\n"
    "v_mfma_f32_16x16x32_bf16 v[40:43], a[136:139], v[104:107], v[40:43]\n"
    "v_mfma_f32_16x16x32_bf16 v[44:47], a[140:143], v[104:107], v[44:47]\n"
    "s_waitcnt lgkmcnt(4)\n"
    "v_mfma_f32_16x16x32_bf16 v[48:51], a[128:131], v[108:111], v[48:51]\n"
    "s_add_u32 m0, s48, 28672\n"
    "s_add_u32 s60, s57, 110720\n"
    "buffer_load_dwordx4 v153, s[40:43], s60 offen lds\n"
    "v_mfma_f32_16x16x32_bf16 v[52:55], a[132:135], v[108:111], v[52:55]\n"
    "v_mfma_f32_16x16x32_bf16 v[56:59], a[136:139], v[108:111], v[56:59]\n"
    "v_mfma_f32_16x16x32_bf16 v[60:63], a[140:143], v[108:111], v[60:63]\n"
    "s_waitcnt lgkmcnt(3)\n"
    "v_mfma_f32_16x16x32_bf16 v[0:3], a[144:147], v[112:115], v[0:3]\n"
    "v_mfma_f32_16x16x32_bf16 v[4:7], a[148:151], v[112:115], v[4:7]\n"
    "ds_read_b128 v[96:99], v130 offset:32768\n"
    "v_mfma_f32_16x16x32_bf16 v[8:11], a[152:155], v[112:115], v[8:11]\n"
    "ds_read_b128 v[100:103], v130 offset:33280\n"
    "v_mfma_f32_16x16x32_bf16 v[12:15], a[156:159], v[112:115], v[12:15]\n"
    "ds_read_b128 v[104:107], v130 offset:36864\n"
    "s_waitcnt lgkmcnt(5)\n"
    "v_mfma_f32_16x16x32_bf16 v[16:19], a[144:147], v[116:119], v[16:19]\n"
    "ds_read_b128 v[108:111], v130 offset:37376\n"
    "v_mfma_f32_16x16x32_bf16 v[20:23], a[148:151], v[116:119], v[20:23]\n"
    "v_mfma_f32_16x16x32_bf16 v[24:27], a[152:155], v[116:119], v[24:27]\n"
    "s_add_u32 m0, s48, 32768\n"
    "s_add_u32 s60, s57, 256\n"
    "buffer_load_dwordx4 v153, s[40:43], s60 offen lds\n"
    "v_mfma_f32_16x16x32_bf16 v[28:31], a[156:159], v[116:119], v[28:31]\n"
    "s_waitcnt lgkmcnt(5)\n"
    "v_mfma_f32_16x16x32_bf16 v[32:35], a[144:147], v[120:123], v[32:35]\n"
    "v_mfma_f32_16x16x32_bf16 v[36:39], a[148:151], v[120:123], v[36:39]\n"
    "v_mfma_f32_16x16x32_bf16 v[40:43], a[152:155], v[120:123], v[40:43]\n"
    "v_mfma_f32_16x16x32_bf16 v[44:47], a[156:159], v[120:123], v[44:47]\n"
    "s_waitcnt lgkmcnt(4)\n"
    "v_mfma_f32_16x16x32_bf16 v[48:51], a[144:147], v[124:127], v[48:51]\n"
    "s_add_u32 m0, s48, 36864\n"
    "s_add_u32 s60, s57, 37120\n"
    "buffer_load_dwordx4 v153, s[40:43], s60 offen lds\n"
    "v_mfma_f32_16x16x32_bf16 v[52:55], a[148:151], v[124:127], v[52:55]\n"
    "v_mfma_f32_16x16x32_bf16 v[56:59], a[152:155], v[124:127], v[56:59]\n"
    "v_mfma_f32_16x16x32_bf16 v[60:63], a[156:159], v[124:127], v[60:63]\n"
    "s_waitcnt lgkmcnt(3)\n"
    "v_mfma_f32_16x16x32_bf16 v[0:3], a[160:163], v[96:99], v[0:3]\n"
    "v_mfma_f32_16x16x32_bf16 v[4:7], a[164:167], v[96:99], v[4:7]\n"
    "ds_read_b128 v[112:115], v131 offset:32768\n"
    "v_mfma_f32_16x16x32_bf16 v[8:11], a[168:171], v[96:99], v[8:11]\n"
    "ds_read_b128 v[116:119], v131 offset:33280\n"
    "v_mfma_f32_16x16x32_bf16 v[12:15], a[172:175], v[96:99], v[12:15]\n"
    "ds_read_b128 v[120:123], v131 offset:36864\n"
    "s_waitcnt lgkmcnt(5)\n"
    "v_mfma_f32_16x16x32_bf16 v[16:19], a[160:163], v[100:103], v[16:19]\n"
    "ds_read_b128 v[124:127], v131 offset:37376\n"
    "v_mfma_f32_16x16x32_bf16 v[20:23], a[164:167], v[100:103], v[20:23]\n"
    "v_mfma_f32_16x16x32_bf16 v[24:27], a[168:171], v[100:103], v[24:27]\n"
    "s_add_u32 m0, s48, 40960\n"
    "s_add_u32 s60, s57, 73984\n"
    "buffer_load_dwordx4 v153, s[40:43], s60 offen lds\n"
    "v_mfma_f32_16x16x32_bf16 v[28:31], a[172:175], v[100:103], v[28:31]\n"
    "s_waitcnt lgkmcnt(5)\n"
    "v_mfma_f32_16x16x32_bf16 v[32:35], a[160:163], v[104:107], v[32:35]\n"
    "v_mfma_f32_16x16x32_bf16 v[36:39], a[164:167], v[104:107], v[36:39]\n"
    "v_mfma_f32_16x16x32_bf16 v[40:43], a[168:171], v[104:107], v[40:43]\n"
    "v_mfma_f32_16x16x32_bf16 v[44:47], a[172:175], v[104:107], v[44:47]\n"
    "s_waitcnt lgkmcnt(4)\n"
    "v_mfma_f32_16x16x32_bf16 v[48:51], a[160:163], v[108:111], v[48:51]\n"
    "s_add_u32 m0, s48, 45056\n"
    "s_add_u32 s60, s57, 110848\n"
    "buffer_load_dwordx4 v153, s[40:43], s60 offen lds\n"
    "v_mfma_f32_16x16x32_bf16 v[52:55], a[164:167], v[108:111], v[52:55]\n"
    "v_mfma_f32_16x16x32_bf16 v[56:59], a[168:171], v[108:111], v[56:59]\n"
    "v_mfma_f32_16x16x32_bf16 v[60:63], a[172:175], v[108:111], v[60:63]\n"
    "s_waitcnt lgkmcnt(0)\n"
    "s_waitcnt vmcnt(12)\n"
    "s_barrier\n"
    "s_mov_b64 exec, s[62:63]\n"
    "buffer_store_dword v152, v152, s[36:39], s59 offen sc1\n"
    "s_mov_b64 exec, -1\n"
    "v_mfma_f32_16x16x32_bf16 v[0:3], a[176:179], v[112:115], v[0:3]\n"
    "v_mfma_f32_16x16x32_bf16 v[4:7], a[180:183], v[112:115], v[4:7]\n"
    "ds_read_b128 v[96:99], v132\n"
    "v_mfma_f32_16x16x32_bf16 v[8:11], a[184:187], v[112:115], v[8:11]\n"
    "ds_read_b128 v[100:103], v132 offset:512\n"
    "v_mfma_f32_16x16x32_bf16 v[12:15], a[188:191], v[112:115], v[12:15]\n"
    "ds_read_b128 v[104:107], v132 offset:4096\n"
    "v_mfma_f32_16x16x32_bf16 v[16:19], a[176:179], v[116:119], v[16:19]\n"
    "ds_read_b128 v[108:111], v132 offset:4608\n"
    "v_mfma_f32_16x16x32_bf16 v[20:23], a[180:183], v[116:119], v[20:23]\n"
    "v_mfma_f32_16x16x32_bf16 v[24:27], a[184:187], v[116:119], v[24:27]\n"
    "s_add_u32 m0, s48, 49152\n"
    "s_add_u32 s60, s57, 384\n"
    "buffer_load_dwordx4 v153, s[40:43], s60 offen lds\n"
    "v_mfma_f32_16x16x32_bf16 v[28:31], a[188:191], v[116:119], v[28:31]\n"
    "v_mfma_f32_16x16x32_bf16 v[32:35], a[176:179], v[120:123], v[32:35]\n"
    "v_mfma_f32_16x16x32_bf16 v[36:39], a[180:183], v[120:123], v[36:39]\n"
    "v_mfma_f32_16x16x32_bf16 v[40:43], a[184:187], v[120:123], v[40:43]\n"
    "v_mfma_f32_16x16x32_bf16 v[44:47], a[188:191], v[120:123], v[44:47]\n"
    "v_mfma_f32_16x16x32_bf16 v[48:51], a[176:179], v[124:127], v[48:51]\n"
    "s_add_u32 m0, s48, 53248\n"
    "s_add_u32 s60, s57, 37248\n"
    "buffer_load_dwordx4 v153, s[40:43], s60 offen lds\n"
    "v_mfma_f32_16x16x32_bf16 v[52:55], a[180:183], v[124:127], v[52:55]\n"
    "v_mfma_f32_16x16x32_bf16 v[56:59], a[184:187], v[124:127], v[56:59]\n"
    "v_mfma_f32_16x16x32_bf16 v[60:63], a[188:191], v[124:127], v[60:63]\n"
    "s_waitcnt lgkmcnt(3)\n"
    "v_mfma_f32_16x16x32_bf16 v[0:3], a[192:195], v[96:99], v[0:3]\n"
    "v_mfma_f32_16x16x32_bf16 v[4:7], a[196:199], v[96:99], v[4:7]\n"
    "ds_read_b128 v[112:115], v133\n"
    "v_mfma_f32_16x16x32_bf16 v[8:11], a[200:203], v[96:99], v[8:11]\n"
    "ds_read_b128 v[116:119], v133 offset:512\n"
    "v_mfma_f32_16x16x32_bf16 v[12:15], a[204:207], v[96:99], v[12:15]\n"
    "ds_read_b128 v[120:123], v133 offset:4096\n"
    "s_waitcnt lgkmcnt(5)\n"
    "v_mfma_f32_16x16x32_bf16 v[16:19], a[192:195], v[100:103], v[16:19]\n"
    "ds_read_b128 v[124:127], v133 offset:4608\n"
    "v_mfma_f32_16x16x32_bf16 v[20:23], a[196:199], v[100:103], v[20:23]\n"
    "v_mfma_f32_16x16x32_bf16 v[24:27], a[200:203], v[100:103], v[24:27]\n"
    "s_add_u32 m0, s48, 57344\n"
    "s_add_u32 s60, s57, 74112\n"
    "buffer_load_dwordx4 v153, s[40:43], s60 offen lds\n"
    "v_mfma_f32_16x16x32_bf16 v[28:31], a[204:207], v[100:103], v[28:31]\n"
    "s_waitcnt lgkmcnt(5)\n"
    "v_mfma_f32_16x16x32_bf16 v[32:35], a[192:195], v[104:107], v[32:35]\n"
    "v_mfma_f32_16x16x32_bf16 v[36:39], a[196:199], v[104:107], v[36:39]\n"
    "v_mfma_f32_16x16x32_bf16 v[40:43], a[200:203], v[104:107], v[40:43]\n"
    "v_mfma_f32_16x16x32_bf16 v[44:47], a[204:207], v[104:107], v[44:47]\n"
    "s_waitcnt lgkmcnt(4)\n"
    "v_mfma_f32_16x16x32_bf16 v[48:51], a[192:195], v[108:111], v[48:51]\n"
    "s_add_u32 m0, s48, 61440\n"
    "s_add_u32 s60, s57, 110976\n"
    "buffer_load_dwordx4 v153, s[40:43], s60 offen lds\n"
    "v_mfma_f32_16x16x32_bf16 v[52:55], a[196:199], v[108:111], v[52:55]\n"
    "v_mfma_f32_16x16x32_bf16 v[56:59], a[200:203], v[108:111], v[56:59]\n"
    "v_mfma_f32_16x16x32_bf16 v[60:63], a[204:207], v[108:111], v[60:63]\n"
    "s_waitcnt lgkmcnt(3)\n"
    "v_mfma_f32_16x16x32_bf16 v[0:3], a[208:211], v[112:115], v[0:3]\n"
    "v_mfma_f32_16x16x32_bf16 v[4:7], a[212:215], v[112:115], v[4:7]\n"
    "ds_read_b128 v[96:99], v132 offset:16384\n"
    "v_mfma_f32_16x16x32_bf16 v[8:11], a[216:219], v[112:115], v[8:11]\n"
    "ds_read_b128 v[100:103], v132 offset:16896\n"
    "v_mfma_f32_16x16x32_bf16 v[12:15], a[220:223], v[112:115], v[12:15]\n"
    "ds_read_b128 v[104:107], v132 offset:20480\n"
    "s_waitcnt lgkmcnt(5)\n"
    "v_mfma_f32_16x16x32_bf16 v[16:19], a[208:211], v[116:119], v[16:19]\n"
    "ds_read_b128 v[108:111], v132 offset:20992\n"
    "v_mfma_f32_16x16x32_bf16 v[20:23], a[212:215], v[116:119], v[20:23]\n"
    "v_mfma_f32_16x16x32_bf16 v[24:27], a[216:219], v[116:119], v[24:27]\n"
    "s_add_u32 m0, s48, 65536\n"
    "s_add_u32 s60, s57, 512\n"
    "buffer_load_dwordx4 v153, s[40:43], s60 offen lds\n"
    "v_mfma_f32_16x16x32_bf16 v[28:31], a[220:223], v[116:119], v[28:31]\n"
    "s_waitcnt lgkmcnt(5)\n"
    "v_mfma_f32_16x16x32_bf16 v[32:35], a[208:211], v[120:123], v[32:35]\n"
    "v_mfma_f32_16x16x32_bf16 v[36:39], a[212:215], v[120:123], v[36:39]\n"
    "v_mfma_f32_16x16x32_bf16 v[40:43], a[216:219], v[120:123], v[40:43]\n"
    "v_mfma_f32_16x16x32_bf16 v[44:47], a[220:223], v[120:123], v[44:47]\n"
    "s_waitcnt lgkmcnt(4)\n"
    "v_mfma_f32_16x16x32_bf16 v[48:51], a[208:211], v[124:127], v[48:51]\n"
    "s_add_u32 m0, s48, 69632\n"
    "s_add_u32 s60, s57, 37376\n"
    "buffer_load_dwordx4 v153, s[40:43], s60 offen lds\n"
    "v_mfma_f32_16x16x32_bf16 v[52:55], a[212:215], v[124:127], v[52:55]\n"
    "v_mfma_f32_16x16x32_bf16 v[56:59], a[216:219], v[124:127], v[56:59]\n"
    "v_mfma_f32_16x16x32_bf16 v[60:63], a[220:223], v[124:127], v[60:63]\n"
    "s_waitcnt lgkmcnt(3)\n"
    "v_mfma_f32_16x16x32_bf16 v[0:3], a[224:227], v[96:99], v[0:3]\n"
    "v_mfma_f32_16x16x32_bf16 v[4:7], a[228:231], v[96:99], v[4:7]\n"
    "ds_read_b128 v[112:115], v133 offset:16384\n"
    "v_mfma_f32_16x16x32_bf16 v[8:11], a[232:235], v[96:99], v[8:11]\n"
    "ds_read_b128 v[116:119], v133 offset:16896\n"
    "v_mfma_f32_16x16x32_bf16 v[12:15], a[236:239], v[96:99], v[12:15]\n"
    "ds_read_b128 v[120:123], v133 offset:20480\n"
    "s_waitcnt lgkmcnt(5)\n"
    "v_mfma_f32_16x16x32_bf16 v[16:19], a[224:227], v[100:103], v[16:19]\n"
    "ds_read_b128 v[124:127], v133 offset:20992\n"
    "v_mfma_f32_16x16x32_bf16 v[20:23], a[228:231], v[100:103], v[20:23]\n"
    "v_mfma_f32_16x16x32_bf16 v[24:27], a[232:235], v[100:103], v[24:27]\n"
    "s_add_u32 m0, s48, 73728\n"
    "s_add_u32 s60, s57, 74240\n"
    "buffer_load_dwordx4 v153, s[40:43], s60 offen lds\n"
    "v_mfma_f32_16x16x32_bf16 v[28:31], a[236:239], v[100:103], v[28:31]\n"
    "s_waitcnt lgkmcnt(5)\n"
    "v_mfma_f32_16x16x32_bf16 v[32:35], a[224:227], v[104:107], v[32:35]\n"
    "v_mfma_f32_16x16x32_bf16 v[36:39], a[228:231], v[104:107], v[36:39]\n"
    "v_mfma_f32_16x16x32_bf16 v[40:43], a[232:235], v[104:107], v[40:43]\n"
    "v_mfma_f32_16x16x32_bf16 v[44:47], a[236:239], v[104:107], v[44:47]\n"
    "s_waitcnt lgkmcnt(4)\n"
    "v_mfma_f32_16x16x32_bf16 v[48:51], a[224:227], v[108:111], v[48:51]\n"
    "s_add_u32 m0, s48, 77824\n"
    "s_add_u32 s60, s57, 111104\n"
    "buffer_load_dwordx4 v153, s[40:43], s60 offen lds\n"
    "v_mfma_f32_16x16x32_bf16 v[52:55], a[228:231], v[108:111], v[52:55]\n"
    "v_mfma_f32_16x16x32_bf16 v[56:59], a[232:235], v[108:111], v[56:59]\n"
    "v_mfma_f32_16x16x32_bf16 v[60:63], a[236:239], v[108:111], v[60:63]\n"
    "s_waitcnt lgkmcnt(3)\n"
    "v_mfma_f32_16x16x32_bf16 v[0:3], a[240:243], v[112:115], v[0:3]\n"
    "v_mfma_f32_16x16x32_bf16 v[4:7], a[244:247], v[112:115], v[4:7]\n"
    "ds_read_b128 v[96:99], v132 offset:32768\n"
    "v_mfma_f32_16x16x32_bf16 v[8:11], a[248:251], v[112:115], v[8:11]\n"
    "ds_read_b128 v[100:103], v132 offset:33280\n"
    "v_mfma_f32_16x16x32_bf16 v[12:15], a[252:255], v[112:115], v[12:15]\n"
    "ds_read_b128 v[104:107], v132 offset:36864\n"
    "s_waitcnt lgkmcnt(5)\n"
    "v_mfma_f32_16x16x32_bf16 v[16:19], a[240:243], v[116:119], v[16:19]\n"
    "ds_read_b128 v[108:111], v132 offset:37376\n"
    "v_mfma_f32_16x16x32_bf16 v[20:23], a[244:247], v[116:119], v[20:23]\n"
    "v_mfma_f32_16x16x32_bf16 v[24:27], a[248:251], v[116:119], v[24:27]\n"
    "s_add_u32 m0, s48, 81920\n"
    "s_add_u32 s60, s57, 640\n"
    "buffer_load_dwordx4 v153, s[40:43], s60 offen lds\n"
    "v_mfma_f32_16x16x32_bf16 v[28:31], a[252:255], v[116:119], v[28:31]\n"
    "s_waitcnt lgkmcnt(5)\n"
    "v_mfma_f32_16x16x32_bf16 v[32:35], a[240:243], v[120:123], v[32:35]\n"
    "v_mfma_f32_16x16x32_bf16 v[36:39], a[244:247], v[120:123], v[36:39]\n"
    "v_mfma_f32_16x16x32_bf16 v[40:43], a[248:251], v[120:123], v[40:43]\n"
    "v_mfma_f32_16x16x32_bf16 v[44:47], a[252:255], v[120:123], v[44:47]\n"
    "s_waitcnt lgkmcnt(4)\n"
    "v_mfma_f32_16x16x32_bf16 v[48:51], a[240:243], v[124:127], v[48:51]\n"
    "s_add_u32 m0, s48, 86016\n"
    "s_add_u32 s60, s57, 37504\n"
    "buffer_load_dwordx4 v153, s[40:43], s60 offen lds\n"
    "v_mfma_f32_16x16x32_bf16 v[52:55], a[244:247], v[124:127], v[52:55]\n"
    "v_mfma_f32_16x16x32_bf16 v[56:59], a[248:251], v[124:127], v[56:59]\n"
    "v_mfma_f32_16x16x32_bf16 v[60:63], a[252:255], v[124:127], v[60:63]\n"
    "s_waitcnt lgkmcnt(3)\n"
    "v_mfma_f32_16x16x32_bf16 v[0:3], v[160:163], v[96:99], v[0:3]\n"
    "v_mfma_f32_16x16x32_bf16 v[4:7], v[164:167], v[96:99], v[4:7]\n"
    "ds_read_b128 v[112:115], v133 offset:32768\n"
    "v_mfma_f32_16x16x32_bf16 v[8:11], v[168:171], v[96:99], v[8:11]\n"
    "ds_read_b128 v[116:119], v133 offset:33280\n"
    "v_mfma_f32_16x16x32_bf16 v[12:15], v[172:175], v[96:99], v[12:15]\n"
    "ds_read_b128 v[120:123], v133 offset:36864\n"
    "s_waitcnt lgkmcnt(5)\n"
    "v_mfma_f32_16x16x32_bf16 v[16:19], v[160:163], v[100:103], v[16:19]\n"
    "ds_read_b128 v[124:127], v133 offset:37376\n"
    "v_mfma_f32_16x16x32_bf16 v[20:23], v[164:167], v[100:103], v[20:23]\n"
    "v_mfma_f32_16x16x32_bf16 v[24:27], v[168:171], v[100:103], v[24:27]\n"
    "s_add_u32 m0, s48, 90112\n"
    "s_add_u32 s60, s57, 74368\n"
    "buffer_load_dwordx4 v153, s[40:43], s60 offen lds\n"
    "v_mfma_f32_16x16x32_bf16 v[28:31], v[172:175], v[100:103], v[28:31]\n"
    "s_waitcnt lgkmcnt(5)\n"
    "v_mfma_f32_16x16x32_bf16 v[32:35], v[160:163], v[104:107], v[32:35]\n"
    "v_mfma_f32_16x16x32_bf16 v[36:39], v[164:167], v[104:107], v[36:39]\n"
    "v_mfma_f32_16x16x32_bf16 v[40:43], v[168:171], v[104:107], v[40:43]\n"
    "v_mfma_f32_16x16x32_bf16 v[44:47], v[172:175], v[104:107], v[44:47]\n"
    "s_waitcnt lgkmcnt(4)\n"
    "v_mfma_f32_16x16x32_bf16 v[48:51], v[160:163], v[108:111], v[48:51]\n"
    "s_add_u32 m0, s48, 94208\n"
    "s_add_u32 s60, s57, 111232\n"
    "buffer_load_dwordx4 v153, s[40:43], s60 offen lds\n"
    "v_mfma_f32_16x16x32_bf16 v[52:55], v[164:167], v[108:111], v[52:55]\n"
    "v_mfma_f32_16x16x32_bf16 v[56:59], v[168:171], v[108:111], v[56:59]\n"
    "v_mfma_f32_16x16x32_bf16 v[60:63], v[172:175], v[108:111], v[60:63]\n"
    "s_waitcnt lgkmcnt(0)\n"
    "s_waitcnt vmcnt(12)\n"
    "s_barrier\n"
    "v_mfma_f32_16x16x32_bf16 v[0:3], v[176:179], v[112:115], v[0:3]\n"
    "v_mfma_f32_16x16x32_bf16 v[4:7], v[180:183], v[112:115], v[4:7]\n"
    "ds_read_b128 v[96:99], v128\n"
    "v_mfma_f32_16x16x32_bf16 v[8:11], v[184:187], v[112:115], v[8:11]\n"
    "ds_read_b128 v[100:103], v128 offset:512\n"
    "v_mfma_f32_16x16x32_bf16 v[12:15], v[188:191], v[112:115], v[12:15]\n"
    "ds_read_b128 v[104:107], v128 offset:4096\n"
    "v_mfma_f32_16x16x32_bf16 v[16:19], v[176:179], v[116:119], v[16:19]\n"
    "ds_read_b128 v[108:111], v128 offset:4608\n"
    "v_mfma_f32_16x16x32_bf16 v[20:23], v[180:183], v[116:119], v[20:23]\n"
    "v_mfma_f32_16x16x32_bf16 v[24:27], v[184:187], v[116:119], v[24:27]\n"
    "s_add_u32 m0, s48, 98304\n"
    "s_add_u32 s60, s57, 768\n"
    "buffer_load_dwordx4 v153, s[40:43], s60 offen lds\n"
    "v_mfma_f32_16x16x32_bf16 v[28:31], v[188:191], v[116:119], v[28:31]\n"
    "v_mfma_f32_16x16x32_bf16 v[32:35], v[176:179], v[120:123], v[32:35]\n"
    "v_mfma_f32_16x16x32_bf16 v[36:39], v[180:183], v[120:123], v[36:39]\n"
    "v_mfma_f32_16x16x32_bf16 v[40:43], v[184:187], v[120:123], v[40:43]\n"
    "v_mfma_f32_16x16x32_bf16 v[44:47], v[188:191], v[120:123], v[44:47]\n"
    "v_mfma_f32_16x16x32_bf16 v[48:51], v[176:179], v[124:127], v[48:51]\n"
    "s_add_u32 m0, s48, 102400\n"
    "s_add_u32 s60, s57, 37632\n"
    "buffer_load_dwordx4 v153, s[40:43], s60 offen lds\n"
    "v_mfma_f32_16x16x32_bf16 v[52:55], v[180:183], v[124:127], v[52:55]\n"
    "v_mfma_f32_16x16x32_bf16 v[56:59], v[184:187], v[124:127], v[56:59]\n"
    "v_mfma_f32_16x16x32_bf16 v[60:63], v[188:191], v[124:127], v[60:63]\n"
    "s_nop 3\n"
    "v_cvt_pk_bf16_f32 v64, v0, v1\n"
    "v_cvt_pk_bf16_f32 v65, v2, v3\n"
    "v_cvt_pk_bf16_f32 v66, v4, v5\n"
    "v_cvt_pk_bf16_f32 v67, v6, v7\n"
    "v_cvt_pk_bf16_f32 v68, v16, v17\n"
    "v_cvt_pk_bf16_f32 v69, v18, v19\n"
    "v_cvt_pk_bf16_f32 v70, v20, v21\n"
    "v_cvt_pk_bf16_f32 v71, v22, v23\n"
    "v_cvt_pk_bf16_f32 v72, v8, v9\n"
    "v_cvt_pk_bf16_f32 v73, v10, v11\n"
    "v_cvt_pk_bf16_f32 v74, v12, v13\n"
    "v_cvt_pk_bf16_f32 v75, v14, v15\n"
    "v_cvt_pk_bf16_f32 v76, v24, v25\n"
    "v_cvt_pk_bf16_f32 v77, v26, v27\n"
    "v_cvt_pk_bf16_f32 v78, v28, v29\n"
    "v_cvt_pk_bf16_f32 v79, v30, v31\n"
    "v_cvt_pk_bf16_f32 v80, v32, v33\n"
    "v_cvt_pk_bf16_f32 v81, v34, v35\n"
    "v_cvt_pk_bf16_f32 v82, v36, v37\n"
    "v_cvt_pk_bf16_f32 v83, v38, v39\n"
    "v_cvt_pk_bf16_f32 v84, v48, v49\n"
    "v_cvt_pk_bf16_f32 v85, v50, v51\n"
    "v_cvt_pk_bf16_f32 v86, v52, v53\n"
    "v_cvt_pk_bf16_f32 v87, v54, v55\n"
    "v_cvt_pk_bf16_f32 v88, v40, v41\n"
    "v_cvt_pk_bf16_f32 v89, v42, v43\n"
    "v_cvt_pk_bf16_f32 v90, v44, v45\n"
    "v_cvt_pk_bf16_f32 v91, v46, v47\n"
    "v_cvt_pk_bf16_f32 v92, v56, v57\n"
    "v_cvt_pk_bf16_f32 v93, v58, v59\n"
    "v_cvt_pk_bf16_f32 v94, v60, v61\n"
    "v_cvt_pk_bf16_f32 v95, v62, v63\n"
    "s_mov_b32 s58, s49\n"
    "s_mov_b32 s59, s50\n"
    "s_add_u32 s49, s49, 1048576\n"
    "s_add_u32 s50, s50, s53\n"
    "s_add_u32 s52, s52, 147456\n"
    "s_cmp_eq_u32 s56, 0\n"
    "s_cbranch_scc1 G0I\n"
    "s_cmp_eq_u32 s56, 15\n"
    "s_cbranch_scc0 G0N\n"
    "G0I:\n"
    "s_add_u32 s61, s58, 0\n"
    "buffer_store_dwordx4 v[64:67], v152, s[36:39], s61 offen sc1\n"
    "buffer_store_dwordx4 v[68:71], v152, s[36:39], s61 offen offset:1024 sc1\n"
    "buffer_store_dwordx4 v[72:75], v152, s[36:39], s61 offen offset:2048 sc1\n"
    "buffer_store_dwordx4 v[76:79], v152, s[36:39], s61 offen offset:3072 sc1\n"
    "s_add_u32 s61, s58, 262144\n"
    "buffer_store_dwordx4 v[80:83], v152, s[36:39], s61 offen sc1\n"
    "buffer_store_dwordx4 v[84:87], v152, s[36:39], s61 offen offset:1024 sc1\n"
    "buffer_store_dwordx4 v[88:91], v152, s[36:39], s61 offen offset:2048 sc1\n"
    "buffer_store_dwordx4 v[92:95], v152, s[36:39], s61 offen offset:3072 sc1\n"
    "s_waitcnt vmcnt(0)\n"
    "s_barrier\n"
    "s_mov_b64 exec, s[62:63]\n"
    "buffer_store_dword v152, v152, s[36:39], s59 offen sc1\n"
    "s_mov_b64 exec, -1\n"
    "s_mov_b32 s58, 0x70000000\n"
    "s_mov_b32 s59, 0x70000000\n"
    "G0N:\n"
    "s_add_u32 s56, s56, 1\n"
    "s_cmp_lt_u32 s56, 16\n"
    "s_waitcnt lgkmcnt(0)\n"
    "s_cbranch_scc1 GT0\n"
    "s_branch GX\n"
    "GT1:\n"
    "v_mfma_f32_16x16x32_bf16 v[0:3], v[96:99], a[0:3], 0\n"
    "v_mfma_f32_16x16x32_bf16 v[4:7], v[96:99], a[4:7], 0\n"
    "ds_read_b128 v[112:115], v129\n"
    "v_mfma_f32_16x16x32_bf16 v[8:11], v[96:99], a[8:11], 0\n"
    "ds_read_b128 v[116:119], v129 offset:512\n"
    "v_mfma_f32_16x16x32_bf16 v[12:15], v[96:99], a[12:15], 0\n"
    "ds_read_b128 v[120:123], v129 offset:4096\n"
    "v_mfma_f32_16x16x32_bf16 v[16:19], v[100:103], a[0:3], 0\n"
    "ds_read_b128 v[124:127], v129 offset:4608\n"
    "v_mfma_f32_16x16x32_bf16 v[20:23], v[100:103], a[4:7], 0\n"
    "v_mfma_f32_16x16x32_bf16 v[24:27], v[100:103], a[8:11], 0\n"
    "s_add_u32 m0, s48, 106496\n"
    "s_add_u32 s60, s57, 74496\n"
    "buffer_load_dwordx4 v153, s[40:43], s60 offen lds\n"
    "v_mfma_f32_16x16x32_bf16 v[28:31], v[100:103], a[12:15], 0\n"
    "v_mfma_f32_16x16x32_bf16 v[32:35], v[104:107], a[0:3], 0\n"
    "v_mfma_f32_16x16x32_bf16 v[36:39], v[104:107], a[4:7], 0\n"
    "s_add_u32 s61, s58, 0\n"
    "buffer_store_dwordx4 v[64:67], v152, s[36:39], s61 offen sc1\n"
    "v_mfma_f32_16x16x32_bf16 v[40:43], v[104:107], a[8:11], 0\n"
    "v_mfma_f32_16x16x32_bf16 v[44:47], v[104:107], a[12:15], 0\n"
    "v_mfma_f32_16x16x32_bf16 v[48:51], v[108:111], a[0:3], 0\n"
    "s_add_u32 m0, s48, 110592\n"
    "s_add_u32 s60, s57, 111360\n"
    "buffer_load_dwordx4 v153, s[40:43], s60 offen lds\n"
    "v_mfma_f32_16x16x32_bf16 v[52:55], v[108:111], a[4:7], 0\n"
    "v_mfma_f32_16x16x32_bf16 v[56:59], v[108:111], a[8:11], 0\n"
    "buffer_store_dwordx4 v[68:71], v152, s[36:39], s61 offen offset:1024 sc1\n"
    "v_mfma_f32_16x16x32_bf16 v[60:63], v[108:111], a[12:15], 0\n"
    "s_waitcnt lgkmcnt(3)\n"
    "v_mfma_f32_16x16x32_bf16 v[0:3], v[112:115], a[16:19], v[0:3]\n"
    "v_mfma_f32_16x16x32_bf16 v[4:7], v[112:115], a[20:23], v[4:7]\n"
    "ds_read_b128 v[96:99], v128 offset:16384\n"
    "v_mfma_f32_16x16x32_bf16 v[8:11], v[112:115], a[24:27], v[8:11]\n"
    "ds_read_b128 v[100:103], v128 offset:16896\n"
    "v_mfma_f32_16x16x32_bf16 v[12:15], v[112:115], a[28:31], v[12:15]\n"
    "ds_read_b128 v[104:107], v128 offset:20480\n"
    "s_waitcnt lgkmcnt(5)\n"
    "v_mfma_f32_16x16x32_bf16 v[16:19], v[116:119], a[16:19], v[16:19]\n"
    "ds_read_b128 v[108:111], v128 offset:20992\n"
    "v_mfma_f32_16x16x32_bf16 v[20:23], v[116:119], a[20:23], v[20:23]\n"
    "v_mfma_f32_16x16x32_bf16 v[24:27], v[116:119], a[24:27], v[24:27]\n"
    "s_add_u32 m0, s48, 114688\n"
    "s_add_u32 s60, s57, 896\n"
    "buffer_load_dwordx4 v153, s[40:43], s60 offen lds\n"
    "v_mfma_f32_16x16x32_bf16 v[28:31], v[116:119], a[28:31], v[28:31]\n"
    "s_waitcnt lgkmcnt(5)\n"
    "v_mfma_f32_16x16x32_bf16 v[32:35], v[120:123], a[16:19], v[32:35]\n"
    "v_mfma_f32_16x16x32_bf16 v[36:39], v[120:123], a[20:23], v[36:39]\n"
    "buffer_store_dwordx4 v[72:75], v152, s[36:39], s61 offen offset:2048 sc1\n"
    "v_mfma_f32_16x16x32_bf16 v[40:43], v[120:123], a[24:27], v[40:43]\n"
    "v_mfma_f32_16x16x32_bf16 v[44:47], v[120:123], a[28:31], v[44:47]\n"
    "s_waitcnt lgkmcnt(4)\n"
    "v_mfma_f32_16x16x32_bf16 v[48:51], v[124:127], a[16:19], v[48:51]\n"
    "s_add_u32 m0, s48, 118784\n"
    "s_add_u32 s60, s57, 37760\n"
    "buffer_load_dwordx4 v153, s[40:43], s60 offen lds\n"
    "v_mfma_f32_16x16x32_bf16 v[52:55], v[124:127], a[20:23], v[52:55]\n"
    "v_mfma_f32_16x16x32_bf16 v[56:59], v[124:127], a[24:27], v[56:59]\n"
    "buffer_store_dwordx4 v[76:79], v152, s[36:39], s61 offen offset:3072 sc1\n"
    "v_mfma_f32_16x16x32_bf16 v[60:63], v[124:127], a[28:31], v[60:63]\n"
    "s_waitcnt lgkmcnt(3)\n"
    "v_mfma_f32_16x16x32_bf16 v[0:3], v[96:99], a[32:35], v[0:3]\n"
    "v_mfma_f32_16x16x32_bf16 v[4:7], v[96:99], a[36:39], v[4:7]\n"
    "ds_read_b128 v[112:115], v129 offset:16384\n"
    "v_mfma_f32_16x16x32_bf16 v[8:11], v[96:99], a[40:43], v[8:11]\n"
    "ds_read_b128 v[116:119], v129 offset:16896\n"
    "v_mfma_f32_16x16x32_bf16 v[12:15], v[96:99], a[44:47], v[12:15]\n"
    "ds_read_b128 v[120:123], v129 offset:20480\n"
    "s_waitcnt lgkmcnt(5)\n"
    "v_mfma_f32_16x16x32_bf16 v[16:19], v[100:103], a[32:35], v[16:19]\n"
    "ds_read_b128 v[124:127], v129 offset:20992\n"
    "v_mfma_f32_16x16x32_bf16 v[20:23], v[100:103], a[36:39], v[20:23]\n"
    "v_mfma_f32_16x16x32_bf16 v[24:27], v[100:103], a[40:43], v[24:27]\n"
    "s_add_u32 m0, s48, 122880\n"
    "s_add_u32 s60, s57, 74624\n"
    "buffer_load_dwordx4 v153, s[40:43], s60 offen lds\n"
    "v_mfma_f32_16x16x32_bf16 v[28:31], v[100:103], a[44:47], v[28:31]\n"
    "s_waitcnt lgkmcnt(5)\n"
    "v_mfma_f32_16x16x32_bf16 v[32:35], v[104:107], a[32:35], v[32:35]\n"
    "v_mfma_f32_16x16x32_bf16 v[36:39], v[104:107], a[36:39], v[36:39]\n"
    "s_add_u32 s61, s58, 262144\n"
    "buffer_store_dwordx4 v[80:83], v152, s[36:39], s61 offen sc1\n"
    "v_mfma_f32_16x16x32_bf16 v[40:43], v[104:107], a[40:43], v[40:43]\n"
    "v_mfma_f32_16x16x32_bf16 v[44:47], v[104:107], a[44:47], v[44:47]\n"
    "s_waitcnt lgkmcnt(4)\n"
    "v_mfma_f32_16x16x32_bf16 v[48:51], v[108:111], a[32:35], v[48:51]\n"
    "s_add_u32 m0, s48, 126976\n"
    "s_add_u32 s60, s57, 111488\n"
    "buffer_load_dwordx4 v153, s[40:43], s60 offen lds\n"
    "v_mfma_f32_16x16x32_bf16 v[52:55], v[108:111], a[36:39], v[52:55]\n"
    "v_mfma_f32_16x16x32_bf16 v[56:59], v[108:111], a[40:43], v[56:59]\n"
    "buffer_store_dwordx4 v[84:87], v152, s[36:39], s61 offen offset:1024 sc1\n"
    "v_mfma_f32_16x16x32_bf16 v[60:63], v[108:111], a[44:47], v[60:63]\n"
    "s_waitcnt lgkmcnt(3)\n"
    "v_mfma_f32_16x16x32_bf16 v[0:3], v[112:115], a[48:51], v[0:3]\n"
    "v_mfma_f32_16x16x32_bf16 v[4:7], v[112:115], a[52:55], v[4:7]\n"
    "ds_read_b128 v[96:99], v128 offset:32768\n"
    "v_mfma_f32_16x16x32_bf16 v[8:11], v[112:115], a[56:59], v[8:11]\n"
    "ds_read_b128 v[100:103], v128 offset:33280\n"
    "v_mfma_f32_16x16x32_bf16 v[12:15], v[112:115], a[60:63], v[12:15]\n"
    "ds_read_b128 v[104:107], v128 offset:36864\n"
    "s_waitcnt lgkmcnt(5)\n"
    "v_mfma_f32_16x16x32_bf16 v[16:19], v[116:119], a[48:51], v[16:19]\n"
    "ds_read_b128 v[108:111], v128 offset:37376\n"
    "v_mfma_f32_16x16x32_bf16 v[20:23], v[116:119], a[52:55], v[20:23]\n"
    "v_mfma_f32_16x16x32_bf16 v[24:27], v[116:119], a[56:59], v[24:27]\n"
    "s_add_u32 m0, s48, 131072\n"
    "s_add_u32 s60, s57, 1024\n"
    "buffer_load_dwordx4 v153, s[40:43], s60 offen lds\n"
    "v_mfma_f32_16x16x32_bf16 v[28:31], v[116:119], a[60:63], v[28:31]\n"
    "s_waitcnt lgkmcnt(5)\n"
    "v_mfma_f32_16x16x32_bf16 v[32:35], v[120:123], a[48:51], v[32:35]\n"
    "v_mfma_f32_16x16x32_bf16 v[36:39], v[120:123], a[52:55], v[36:39]\n"
    "buffer_store_dwordx4 v[88:91], v152, s[36:39], s61 offen offset:2048 sc1\n"
    "v_mfma_f32_16x16x32_bf16 v[40:43], v[120:123], a[56:59], v[40:43]\n"
    "v_mfma_f32_16x16x32_bf16 v[44:47], v[120:123], a[60:63], v[44:47]\n"
    "s_waitcnt lgkmcnt(4)\n"
    "v_mfma_f32_16x16x32_bf16 v[48:51], v[124:127], a[48:51], v[48:51]\n"
    "s_add_u32 m0, s48, 135168\n"
    "s_add_u32 s60, s57, 37888\n"
    "buffer_load_dwordx4 v153, s[40:43], s60 offen lds\n"
    "v_mfma_f32_16x16x32_bf16 v[52:55], v[124:127], a[52:55], v[52:55]\n"
    "v_mfma_f32_16x16x32_bf16 v[56:59], v[124:127], a[56:59], v[56:59]\n"
    "buffer_store_dwordx4 v[92:95], v152, s[36:39], s61 offen offset:3072 sc1\n"
    "v_mfma_f32_16x16x32_bf16 v[60:63], v[124:127], a[60:63], v[60:63]\n"
    "s_waitcnt lgkmcnt(3)\n"
    "v_mfma_f32_16x16x32_bf16 v[0:3], v[96:99], a[64:67], v[0:3]\n"
    "v_mfma_f32_16x16x32_bf16 v[4:7], v[96:99], a[68:71], v[4:7]\n"
    "ds_read_b128 v[112:115], v129 offset:32768\n"
    "v_mfma_f32_16x16x32_bf16 v[8:11], v[96:99], a[72:75], v[8:11]\n"
    "ds_read_b128 v[116:119], v129 offset:33280\n"
    "v_mfma_f32_16x16x32_bf16 v[12:15], v[96:99], a[76:79], v[12:15]\n"
    "ds_read_b128 v[120:123], v129 offset:36864\n"
    "s_waitcnt lgkmcnt(5)\n"
    "v_mfma_f32_16x16x32_bf16 v[16:19], v[100:103], a[64:67], v[16:19]\n"
    "ds_read_b128 v[124:127], v129 offset:37376\n"
    "v_mfma_f32_16x16x32_bf16 v[20:23], v[100:103], a[68:71], v[20:23]\n"
    "v_mfma_f32_16x16x32_bf16 v[24:27], v[100:103], a[72:75], v[24:27]\n"
    "s_add_u32 m0, s48, 139264\n"
    "s_add_u32 s60, s57, 74752\n"
    "buffer_load_dwordx4 v153, s[40:43], s60 offen lds\n"
    "v_mfma_f32_16x16x32_bf16 v[28:31], v[100:103], a[76:79], v[28:31]\n"
    "s_waitcnt lgkmcnt(5)\n"
    "v_mfma_f32_16x16x32_bf16 v[32:35], v[104:107], a[64:67], v[32:35]\n"
    "v_mfma_f32_16x16x32_bf16 v[36:39], v[104:107], a[68:71], v[36:39]\n"
    "v_mfma_f32_16x16x32_bf16 v[40:43], v[104:107], a[72:75], v[40:43]\n"
    "v_mfma_f32_16x16x32_bf16 v[44:47], v[104:107], a[76:79], v[44:47]\n"
    "s_waitcnt lgkmcnt(4)\n"
    "v_mfma_f32_16x16x32_bf16 v[48:51], v[108:111], a[64:67], v[48:51]\n"
    "s_add_u32 m0, s48, 143360\n"
    "s_add_u32 s60, s57, 111616\n"
    "buffer_load_dwordx4 v153, s[40:43], s60 offen lds\n"
    "v_mfma_f32_16x16x32_bf16 v[52:55], v[108:111], a[68:71], v[52:55]\n"
    "v_mfma_f32_16x16x32_bf16 v[56:59], v[108:111], a[72:75], v[56:59]\n"
    "v_mfma_f32_16x16x32_bf16 v[60:63], v[108:111], a[76:79], v[60:63]\n"
    "s_waitcnt lgkmcnt(0)\n"
    "s_waitcnt vmcnt(20)\n"
    "s_barrier\n"
    "s_mov_b32 s57, s52\n"
    "v_mfma_f32_16x16x32_bf16 v[0:3], v[112:115], a[80:83], v[0:3]\n"
    "v_mfma_f32_16x16x32_bf16 v[4:7], v[112:115], a[84:87], v[4:7]\n"
    "ds_read_b128 v[96:99], v130\n"
    "v_mfma_f32_16x16x32_bf16 v[8:11], v[112:115], a[88:91], v[8:11]\n"
    "ds_read_b128 v[100:103], v130 offset:512\n"
    "v_mfma_f32_16x16x32_bf16 v[12:15], v[112:115], a[92:95], v[12:15]\n"
    "ds_read_b128 v[104:107], v130 offset:4096\n"
    "v_mfma_f32_16x16x32_bf16 v[16:19], v[116:119], a[80:83], v[16:19]\n"
    "ds_read_b128 v[108:111], v130 offset:4608\n"
    "v_mfma_f32_16x16x32_bf16 v[20:23], v[116:119], a[84:87], v[20:23]\n"
    "v_mfma_f32_16x16x32_bf16 v[24:27], v[116:119], a[88:91], v[24:27]\n"
    "s_add_u32 m0, s48, 0\n"
    "s_add_u32 s60, s57, 0\n"
    "buffer_load_dwordx4 v153, s[40:43], s60 offen lds\n"
    "v_mfma_f32_16x16x32_bf16 v[28:31], v[116:119], a[92:95], v[28:31]\n"
    "v_mfma_f32_16x16x32_bf16 v[32:35], v[120:123], a[80:83], v[32:35]\n"
    "v_mfma_f32_16x16x32_bf16 v[36:39], v[120:123], a[84:87], v[36:39]\n"
    "v_mfma_f32_16x16x32_bf16 v[40:43], v[120:123], a[88:91], v[40:43]\n"
    "v_mfma_f32_16x16x32_bf16 v[44:47], v[120:123], a[92:95], v[44:47]\n"
    "v_mfma_f32_16x16x32_bf16 v[48:51], v[124:127], a[80:83], v[48:51]\n"
    "s_add_u32 m0, s48, 4096\n"
    "s_add_u32 s60, s57, 36864\n"
    "buffer_load_dwordx4 v153, s[40:43], s60 offen lds\n"
    "v_mfma_f32_16x16x32_bf16 v[52:55], v[124:127], a[84:87], v[52:55]\n"
    "v_mfma_f32_16x16x32_bf16 v[56:59], v[124:127], a[88:91], v[56:59]\n"
    "v_mfma_f32_16x16x32_bf16 v[60:63], v[124:127], a[92:95], v[60:63]\n"
    "s_waitcnt lgkmcnt(3)\n"
    "v_mfma_f32_16x16x32_bf16 v[0:3], v[96:99], a[96:99], v[0:3]\n"
    "v_mfma_f32_16x16x32_bf16 v[4:7], v[96:99], a[100:103], v[4:7]\n"
    "ds_read_b128 v[112:115], v131\n"
    "v_mfma_f32_16x16x32_bf16 v[8:11], v[96:99], a[104:107], v[8:11]\n"
    "ds_read_b128 v[116:119], v131 offset:512\n"
    "v_mfma_f32_16x16x32_bf16 v[12:15], v[96:99], a[108:111], v[12:15]\n"
    "ds_read_b128 v[120:123], v131 offset:4096\n"
    "s_waitcnt lgkmcnt(5)\n"
    "v_mfma_f32_16x16x32_bf16 v[16:19], v[100:103], a[96:99], v[16:19]\n"
    "ds_read_b128 v[124:127], v131 offset:4608\n"
    "v_mfma_f32_16x16x32_bf16 v[20:23], v[100:103], a[100:103], v[20:23]\n"
    "v_mfma_f32_16x16x32_bf16 v[24:27], v[100:103], a[104:107], v[24:27]\n"
    "s_add_u32 m0, s48, 8192\n"
    "s_add_u32 s60, s57, 73728\n"
    "buffer_load_dwordx4 v153, s[40:43], s60 offen lds\n"
    "v_mfma_f32_16x16x32_bf16 v[28:31], v[100:103], a[108:111], v[28:31]\n"
    "s_waitcnt lgkmcnt(5)\n"
    "v_mfma_f32_16x16x32_bf16 v[32:35], v[104:107], a[96:99], v[32:35]\n"
    "v_mfma_f32_16x16x32_bf16 v[36:39], v[104:107], a[100:103], v[36:39]\n"
    "v_mfma_f32_16x16x32_bf16 v[40:43], v[104:107], a[104:107], v[40:43]\n"
    "v_mfma_f32_16x16x32_bf16 v[44:47], v[104:107], a[108:111], v[44:47]\n"
    "s_waitcnt lgkmcnt(4)\n"
    "v_mfma_f32_16x16x32_bf16 v[48:51], v[108:111], a[96:99], v[48:51]\n"
    "s_add_u32 m0, s48, 12288\n"
    "s_add_u32 s60, s57, 110592\n"
    "buffer_load_dwordx4 v153, s[40:43], s60 offen lds\n"
    "v_mfma_f32_16x16x32_bf16 v[52:55], v[108:111], a[100:103], v[52:55]\n"
    "v_mfma_f32_16x16x32_bf16 v[56:59], v[108:111], a[104:107], v[56:59]\n"
    "v_mfma_f32_16x16x32_bf16 v[60:63], v[108:111], a[108:111], v[60:63]\n"
    "s_waitcnt lgkmcnt(3)\n"
    "v_mfma_f32_16x16x32_bf16 v[0:3], v[112:115], a[112:115], v[0:3]\n"
    "v_mfma_f32_16x16x32_bf16 v[4:7], v[112:115], a[116:119], v[4:7]\n"
    "ds_read_b128 v[96:99], v130 offset:16384\n"
    "v_mfma_f32_16x16x32_bf16 v[8:11], v[112:115], a[120:123], v[8:11]\n"
    "ds_read_b128 v[100:103], v130 offset:16896\n"
    "v_mfma_f32_16x16x32_bf16 v[12:15], v[112:115], a[124:127], v[12:15]\n"
    "ds_read_b128 v[104:107], v130 offset:20480\n"
    "s_waitcnt lgkmcnt(5)\n"
    "v_mfma_f32_16x16x32_bf16 v[16:19], v[116:119], a[112:115], v[16:19]\n"
    "ds_read_b128 v[108:111], v130 offset:20992\n"
    "v_mfma_f32_16x16x32_bf16 v[20:23], v[116:119], a[116:119], v[20:23]\n"
    "v_mfma_f32_16x16x32_bf16 v[24:27], v[116:119], a[120:123], v[24:27]\n"
    "s_add_u32 m0, s48, 16384\n"
    "s_add_u32 s60, s57, 128\n"
    "buffer_load_dwordx4 v153, s[40:43], s60 offen lds\n"
    "v_mfma_f32_16x16x32_bf16 v[28:31], v[116:119], a[124:127], v[28:31]\n"
    "s_waitcnt lgkmcnt(5)\n"
    "v_mfma_f32_16x16x32_bf16 v[32:35], v[120:123], a[112:115], v[32:35]\n"
    "v_mfma_f32_16x16x32_bf16 v[36:39], v[120:123], a[116:119], v[36:39]\n"
    "v_mfma_f32_16x16x32_bf16 v[40:43], v[120:123], a[120:123], v[40:43]\n"
    "v_mfma_f32_16x16x32_bf16 v[44:47], v[120:123], a[124:127], v[44:47]\n"
    "s_waitcnt lgkmcnt(4)\n"
    "v_mfma_f32_16x16x32_bf16 v[48:51], v[124:127], a[112:115], v[48:51]\n"
    "s_add_u32 m0, s48, 20480\n"
    "s_add_u32 s60, s57, 36992\n"
    "buffer_load_dwordx4 v153, s[40:43], s60 offen lds\n"
    "v_mfma_f32_16x16x32_bf16 v[52:55], v[124:127], a[116:119], v[52:55]\n"
    "v_mfma_f32_16x16x32_bf16 v[56:59], v[124:127], a[120:123], v[56:59]\n"
    "v_mfma_f32_16x16x32_bf16 v[60:63], v[124:127], a[124:127], v[60:63]\n"
    "s_waitcnt lgkmcnt(3)\n"
    "v_mfma_f32_16x16x32_bf16 v[0:3], v[96:99], a[128:131], v[0:3]\n"
    "v_mfma_f32_16x16x32_bf16 v[4:7], v[96:99], a[132:135], v[4:7]\n"
    "ds_read_b128 v[112:115], v131 offset:16384\n"
    "v_mfma_f32_16x16x32_bf16 v[8:11], v[96:99], a[136:139], v[8:11]\n"
    "ds_read_b128 v[116:119], v131 offset:16896\n"
    "v_mfma_f32_16x16x32_bf16 v[12:15], v[96:99], a[140:143], v[12:15]\n"
    "ds_read_b128 v[120:123], v131 offset:20480\n"
    "s_waitcnt lgkmcnt(5)\n"
    "v_mfma_f32_16x16x32_bf16 v[16:19], v[100:103], a[128:131], v[16:19]\n"
    "ds_read_b128 v[124:127], v131 offset:20992\n"
    "v_mfma_f32_16x16x32_bf16 v[20:23], v[100:103], a[132:135], v[20:23]\n"
    "v_mfma_f32_16x16x32_bf16 v[24:27], v[100:103], a[136:139], v[24:27]\n"
    "s_add_u32 m0, s48, 24576\n"
    "s_add_u32 s60, s57, 73856\n"
    "buffer_load_dwordx4 v153, s[40:43], s60 offen lds\n"
    "v_mfma_f32_16x16x32_bf16 v[28:31], v[100:103], a[140:143], v[28:31]\n"
    "s_waitcnt lgkmcnt(5)\n"
    "v_mfma_f32_16x16x32_bf16 v[32:35], v[104:107], a[128:131], v[32:35]\n"
    "v_mfma_f32_16x16x32_bf16 v[36:39], v[104:107], a[132:135], v[36:39]\n"
    "v_mfma_f32_16x16x32_bf16 v[40:43], v[104:107], a[136:139], v[40:43]\n"
    "v_mfma_f32_16x16x32_bf16 v[44:47], v[104:107], a[140:143], v[44:47]\n"
    "s_waitcnt lgkmcnt(4)\n"
    "v_mfma_f32_16x16x32_bf16 v[48:51], v[108:111], a[128:131], v[48:51]\n"
    "s_add_u32 m0, s48, 28672\n"
    "s_add_u32 s60, s57, 110720\n"
    "buffer_load_dwordx4 v153, s[40:43], s60 offen lds\n"
    "v_mfma_f32_16x16x32_bf16 v[52:55], v[108:111], a[132:135], v[52:55]\n"
    "v_mfma_f32_16x16x32_bf16 v[56:59], v[108:111], a[136:139], v[56:59]\n"
    "v_mfma_f32_16x16x32_bf16 v[60:63], v[108:111], a[140:143], v[60:63]\n"
    "s_waitcnt lgkmcnt(3)\n"
    "v_mfma_f32_16x16x32_bf16 v[0:3], v[112:115], a[144:147], v[0:3]\n"
    "v_mfma_f32_16x16x32_bf16 v[4:7], v[112:115], a[148:151], v[4:7]\n"
    "ds_read_b128 v[96:99], v130 offset:32768\n"
    "v_mfma_f32_16x16x32_bf16 v[8:11], v[112:115], a[152:155], v[8:11]\n"
    "ds_read_b128 v[100:103], v130 offset:33280\n"
    "v_mfma_f32_16x16x32_bf16 v[12:15], v[112:115], a[156:159], v[12:15]\n"
    "ds_read_b128 v[104:107], v130 offset:36864\n"
    "s_waitcnt lgkmcnt(5)\n"
    "v_mfma_f32_16x16x32_bf16 v[16:19], v[116:119], a[144:147], v[16:19]\n"
    "ds_read_b128 v[108:111], v130 offset:37376\n"
    "v_mfma_f32_16x16x32_bf16 v[20:23], v[116:119], a[148:151], v[20:23]\n"
    "v_mfma_f32_16x16x32_bf16 v[24:27], v[116:119], a[152:155], v[24:27]\n"
    "s_add_u32 m0, s48, 32768\n"
    "s_add_u32 s60, s57, 256\n"
    "buffer_load_dwordx4 v153, s[40:43], s60 offen lds\n"
    "v_mfma_f32_16x16x32_bf16 v[28:31], v[116:119], a[156:159], v[28:31]\n"
    "s_waitcnt lgkmcnt(5)\n"
    "v_mfma_f32_16x16x32_bf16 v[32:35], v[120:123], a[144:147], v[32:35]\n"
    "v_mfma_f32_16x16x32_bf16 v[36:39], v[120:123], a[148:151], v[36:39]\n"
    "v_mfma_f32_16x16x32_bf16 v[40:43], v[120:123], a[152:155], v[40:43]\n"
    "v_mfma_f32_16x16x32_bf16 v[44:47], v[120:123], a[156:159], v[44:47]\n"
    "s_waitcnt lgkmcnt(4)\n"
    "v_mfma_f32_16x16x32_bf16 v[48:51], v[124:127], a[144:147], v[48:51]\n"
    "s_add_u32 m0, s48, 36864\n"
    "s_add_u32 s60, s57, 37120\n"
    "buffer_load_dwordx4 v153, s[40:43], s60 offen lds\n"
    "v_mfma_f32_16x16x32_bf16 v[52:55], v[124:127], a[148:151], v[52:55]\n"
    "v_mfma_f32_16x16x32_bf16 v[56:59], v[124:127], a[152:155], v[56:59]\n"
    "v_mfma_f32_16x16x32_bf16 v[60:63], v[124:127], a[156:159], v[60:63]\n"
    "s_waitcnt lgkmcnt(3)\n"
    "v_mfma_f32_16x16x32_bf16 v[0:3], v[96:99], a[160:163], v[0:3]\n"
    "v_mfma_f32_16x16x32_bf16 v[4:7], v[96:99], a[164:167], v[4:7]\n"
    "ds_read_b128 v[112:115], v131 offset:32768\n"
    "v_mfma_f32_16x16x32_bf16 v[8:11], v[96:99], a[168:171], v[8:11]\n"
    "ds_read_b128 v[116:119], v131 offset:33280\n"
    "v_mfma_f32_16x16x32_bf16 v[12:15], v[96:99], a[172:175], v[12:15]\n"
    "ds_read_b128 v[120:123], v131 offset:36864\n"
    "s_waitcnt lgkmcnt(5)\n"
    "v_mfma_f32_16x16x32_bf16 v[16:19], v[100:103], a[160:163], v[16:19]\n"
    "ds_read_b128 v[124:127], v131 offset:37376\n"
    "v_mfma_f32_16x16x32_bf16 v[20:23], v[100:103], a[164:167], v[20:23]\n"
    "v_mfma_f32_16x16x32_bf16 v[24:27], v[100:103], a[168:171], v[24:27]\n"
    "s_add_u32 m0, s48, 40960\n"
    "s_add_u32 s60, s57, 73984\n"
    "buffer_load_dwordx4 v153, s[40:43], s60 offen lds\n"
    "v_mfma_f32_16x16x32_bf16 v[28:31], v[100:103], a[172:175], v[28:31]\n"
    "s_waitcnt lgkmcnt(5)\n"
    "v_mfma_f32_16x16x32_bf16 v[32:35], v[104:107], a[160:163], v[32:35]\n"
    "v_mfma_f32_16x16x32_bf16 v[36:39], v[104:107], a[164:167], v[36:39]\n"
    "v_mfma_f32_16x16x32_bf16 v[40:43], v[104:107], a[168:171], v[40:43]\n"
    "v_mfma_f32_16x16x32_bf16 v[44:47], v[104:107], a[172:175], v[44:47]\n"
    "s_waitcnt lgkmcnt(4)\n"
    "v_mfma_f32_16x16x32_bf16 v[48:51], v[108:111], a[160:163], v[48:51]\n"
    "s_add_u32 m0, s48, 45056\n"
    "s_add_u32 s60, s57, 110848\n"
    "buffer_load_dwordx4 v153, s[40:43], s60 offen lds\n"
    "v_mfma_f32_16x16x32_bf16 v[52:55], v[108:111], a[164:167], v[52:55]\n"
    "v_mfma_f32_16x16x32_bf16 v[56:59], v[108:111], a[168:171], v[56:59]\n"
    "v_mfma_f32_16x16x32_bf16 v[60:63], v[108:111], a[172:175], v[60:63]\n"
    "s_waitcnt lgkmcnt(0)\n"
    "s_waitcnt vmcnt(12)\n"
    "s_barrier\n"
    "s_mov_b64 exec, s[62:63]\n"
    "buffer_store_dword v152, v152, s[36:39], s59 offen sc1\n"
    "s_mov_b64 exec, -1\n"
    "v_mfma_f32_16x16x32_bf16 v[0:3], v[112:115], a[176:179], v[0:3]\n"
    "v_mfma_f32_16x16x32_bf16 v[4:7], v[112:115], a[180:183], v[4:7]\n"
    "ds_read_b128 v[96:99], v132\n"
    "v_mfma_f32_16x16x32_bf16 v[8:11], v[112:115], a[184:187], v[8:11]\n"
    "ds_read_b128 v[100:103], v132 offset:512\n"
    "v_mfma_f32_16x16x32_bf16 v[12:15], v[112:115], a[188:191], v[12:15]\n"
    "ds_read_b128 v[104:107], v132 offset:4096\n"
    "v_mfma_f32_16x16x32_bf16 v[16:19], v[116:119], a[176:179], v[16:19]\n"
    "ds_read_b128 v[108:111], v132 offset:4608\n"
    "v_mfma_f32_16x16x32_bf16 v[20:23], v[116:119], a[180:183], v[20:23]\n"
    "v_mfma_f32_16x16x32_bf16 v[24:27], v[116:119], a[184:187], v[24:27]\n"
    "s_add_u32 m0, s48, 49152\n"
    "s_add_u32 s60, s57, 384\n"
    "buffer_load_dwordx4 v153, s[40:43], s60 offen lds\n"
    "v_mfma_f32_16x16x32_bf16 v[28:31], v[116:119], a[188:191], v[28:31]\n"
    "v_mfma_f32_16x16x32_bf16 v[32:35], v[120:123], a[176:179], v[32:35]\n"
    "v_mfma_f32_16x16x32_bf16 v[36:39], v[120:123], a[180:183], v[36:39]\n"
    "v_mfma_f32_16x16x32_bf16 v[40:43], v[120:123], a[184:187], v[40:43]\n"
    "v_mfma_f32_16x16x32_bf16 v[44:47], v[120:123], a[188:191], v[44:47]\n"
    "v_mfma_f32_16x16x32_bf16 v[48:51], v[124:127], a[176:179], v[48:51]\n"
    "s_add_u32 m0, s48, 53248\n"
    "s_add_u32 s60, s57, 37248\n"
    "buffer_load_dwordx4 v153, s[40:43], s60 offen lds\n"
    "v_mfma_f32_16x16x32_bf16 v[52:55], v[124:127], a[180:183], v[52:55]\n"
    "v_mfma_f32_16x16x32_bf16 v[56:59], v[124:127], a[184:187], v[56:59]\n"
    "v_mfma_f32_16x16x32_bf16 v[60:63], v[124:127], a[188:191], v[60:63]\n"
    "s_waitcnt lgkmcnt(3)\n"
    "v_mfma_f32_16x16x32_bf16 v[0:3], v[96:99], a[192:195], v[0:3]\n"
    "v_mfma_f32_16x16x32_bf16 v[4:7], v[96:99], a[196:199], v[4:7]\n"
    "ds_read_b128 v[112:115], v133\n"
    "v_mfma_f32_16x16x32_bf16 v[8:11], v[96:99], a[200:203], v[8:11]\n"
    "ds_read_b128 v[116:119], v133 offset:512\n"
    "v_mfma_f32_16x16x32_bf16 v[12:15], v[96:99], a[204:207], v[12:15]\n"
    "ds_read_b128 v[120:123], v133 offset:4096\n"
    "s_waitcnt lgkmcnt(5)\n"
    "v_mfma_f32_16x16x32_bf16 v[16:19], v[100:103], a[192:195], v[16:19]\n"
    "ds_read_b128 v[124:127], v133 offset:4608\n"
    "v_mfma_f32_16x16x32_bf16 v[20:23], v[100:103], a[196:199], v[20:23]\n"
    "v_mfma_f32_16x16x32_bf16 v[24:27], v[100:103], a[200:203], v[24:27]\n"
    "s_add_u32 m0, s48, 57344\n"
    "s_add_u32 s60, s57, 74112\n"
    "buffer_load_dwordx4 v153, s[40:43], s60 offen lds\n"
    "v_mfma_f32_16x16x32_bf16 v[28:31], v[100:103], a[204:207], v[28:31]\n"
    "s_waitcnt lgkmcnt(5)\n"
    "v_mfma_f32_16x16x32_bf16 v[32:35], v[104:107], a[192:195], v[32:35]\n"
    "v_mfma_f32_16x16x32_bf16 v[36:39], v[104:107], a[196:199], v[36:39]\n"
    "v_mfma_f32_16x16x32_bf16 v[40:43], v[104:107], a[200:203], v[40:43]\n"
    "v_mfma_f32_16x16x32_bf16 v[44:47], v[104:107], a[204:207], v[44:47]\n"
    "s_waitcnt lgkmcnt(4)\n"
    "v_mfma_f32_16x16x32_bf16 v[48:51], v[108:111], a[192:195], v[48:51]\n"
    "s_add_u32 m0, s48, 61440\n"
    "s_add_u32 s60, s57, 110976\n"
    "buffer_load_dwordx4 v153, s[40:43], s60 offen lds\n"
    "v_mfma_f32_16x16x32_bf16 v[52:55], v[108:111], a[196:199], v[52:55]\n"
    "v_mfma_f32_16x16x32_bf16 v[56:59], v[108:111], a[200:203], v[56:59]\n"
    "v_mfma_f32_16x16x32_bf16 v[60:63], v[108:111], a[204:207], v[60:63]\n"
    "s_waitcnt lgkmcnt(3)\n"
    "v_mfma_f32_16x16x32_bf16 v[0:3], v[112:115], a[208:211], v[0:3]\n"
    "v_mfma_f32_16x16x32_bf16 v[4:7], v[112:115], a[212:215], v[4:7]\n"
    "ds_read_b128 v[96:99], v132 offset:16384\n"
    "v_mfma_f32_16x16x32_bf16 v[8:11], v[112:115], a[216:219], v[8:11]\n"
    "ds_read_b128 v[100:103], v132 offset:16896\n"
    "v_mfma_f32_16x16x32_bf16 v[12:15], v[112:115], a[220:223], v[12:15]\n"
    "ds_read_b128 v[104:107], v132 offset:20480\n"
    "s_waitcnt lgkmcnt(5)\n"
    "v_mfma_f32_16x16x32_bf16 v[16:19], v[116:119], a[208:211], v[16:19]\n"
    "ds_read_b128 v[108:111], v132 offset:20992\n"
    "v_mfma_f32_16x16x32_bf16 v[20:23], v[116:119], a[212:215], v[20:23]\n"
    "v_mfma_f32_16x16x32_bf16 v[24:27], v[116:119], a[216:219], v[24:27]\n"
    "s_add_u32 m0, s48, 65536\n"
    "s_add_u32 s60, s57, 512\n"
    "buffer_load_dwordx4 v153, s[40:43], s60 offen lds\n"
    "v_mfma_f32_16x16x32_bf16 v[28:31], v[116:119], a[220:223], v[28:31]\n"
    "s_waitcnt lgkmcnt(5)\n"
    "v_mfma_f32_16x16x32_bf16 v[32:35], v[120:123], a[208:211], v[32:35]\n"
    "v_mfma_f32_16x16x32_bf16 v[36:39], v[120:123], a[212:215], v[36:39]\n"
    "v_mfma_f32_16x16x32_bf16 v[40:43], v[120:123], a[216:219], v[40:43]\n"
    "v_mfma_f32_16x16x32_bf16 v[44:47], v[120:123], a[220:223], v[44:47]\n"
    "s_waitcnt lgkmcnt(4)\n"
    "v_mfma_f32_16x16x32_bf16 v[48:51], v[124:127], a[208:211], v[48:51]\n"
    "s_add_u32 m0, s48, 69632\n"
    "s_add_u32 s60, s57, 37376\n"
    "buffer_load_dwordx4 v153, s[40:43], s60 offen lds\n"
    "v_mfma_f32_16x16x32_bf16 v[52:55], v[124:127], a[212:215], v[52:55]\n"
    "v_mfma_f32_16x16x32_bf16 v[56:59], v[124:127], a[216:219], v[56:59]\n"
    "v_mfma_f32_16x16x32_bf16 v[60:63], v[124:127], a[220:223], v[60:63]\n"
    "s_waitcnt lgkmcnt(3)\n"
    "v_mfma_f32_16x16x32_bf16 v[0:3], v[96:99], a[224:227], v[0:3]\n"
    "v_mfma_f32_16x16x32_bf16 v[4:7], v[96:99], a[228:231], v[4:7]\n"
    "ds_read_b128 v[112:115], v133 offset:16384\n"
    "v_mfma_f32_16x16x32_bf16 v[8:11], v[96:99], a[232:235], v[8:11]\n"
    "ds_read_b128 v[116:119], v133 offset:16896\n"
    "v_mfma_f32_16x16x32_bf16 v[12:15], v[96:99], a[236:239], v[12:15]\n"
    "ds_read_b128 v[120:123], v133 offset:20480\n"
    "s_waitcnt lgkmcnt(5)\n"
    "v_mfma_f32_16x16x32_bf16 v[16:19], v[100:103], a[224:227], v[16:19]\n"
    "ds_read_b128 v[124:127], v133 offset:20992\n"
    "v_mfma_f32_16x16x32_bf16 v[20:23], v[100:103], a[228:231], v[20:23]\n"
    "v_mfma_f32_16x16x32_bf16 v[24:27], v[100:103], a[232:235], v[24:27]\n"
    "s_add_u32 m0, s48, 73728\n"
    "s_add_u32 s60, s57, 74240\n"
    "buffer_load_dwordx4 v153, s[40:43], s60 offen lds\n"
    "v_mfma_f32_16x16x32_bf16 v[28:31], v[100:103], a[236:239], v[28:31]\n"
    "s_waitcnt lgkmcnt(5)\n"
    "v_mfma_f32_16x16x32_bf16 v[32:35], v[104:107], a[224:227], v[32:35]\n"
    "v_mfma_f32_16x16x32_bf16 v[36:39], v[104:107], a[228:231], v[36:39]\n"
    "v_mfma_f32_16x16x32_bf16 v[40:43], v[104:107], a[232:235], v[40:43]\n"
    "v_mfma_f32_16x16x32_bf16 v[44:47], v[104:107], a[236:239], v[44:47]\n"
    "s_waitcnt lgkmcnt(4)\n"
    "v_mfma_f32_16x16x32_bf16 v[48:51], v[108:111], a[224:227], v[48:51]\n"
    "s_add_u32 m0, s48, 77824\n"
    "s_add_u32 s60, s57, 111104\n"
    "buffer_load_dwordx4 v153, s[40:43], s60 offen lds\n"
    "v_mfma_f32_16x16x32_bf16 v[52:55], v[108:111], a[228:231], v[52:55]\n"
    "v_mfma_f32_16x16x32_bf16 v[56:59], v[108:111], a[232:235], v[56:59]\n"
    "v_mfma_f32_16x16x32_bf16 v[60:63], v[108:111], a[236:239], v[60:63]\n"
    "s_waitcnt lgkmcnt(3)\n"
    "v_mfma_f32_16x16x32_bf16 v[0:3], v[112:115], a[240:243], v[0:3]\n"
    "v_mfma_f32_16x16x32_bf16 v[4:7], v[112:115], a[244:247], v[4:7]\n"
    "ds_read_b128 v[96:99], v132 offset:32768\n"
    "v_mfma_f32_16x16x32_bf16 v[8:11], v[112:115], a[248:251], v[8:11]\n"
    "ds_read_b128 v[100:103], v132 offset:33280\n"
    "v_mfma_f32_16x16x32_bf16 v[12:15], v[112:115], a[252:255], v[12:15]\n"
    "ds_read_b128 v[104:107], v132 offset:36864\n"
    "s_waitcnt lgkmcnt(5)\n"
    "v_mfma_f32_16x16x32_bf16 v[16:19], v[116:119], a[240:243], v[16:19]\n"
    "ds_read_b128 v[108:111], v132 offset:37376\n"
    "v_mfma_f32_16x16x32_bf16 v[20:23], v[116:119], a[244:247], v[20:23]\n"
    "v_mfma_f32_16x16x32_bf16 v[24:27], v[116:119], a[248:251], v[24:27]\n"
    "s_add_u32 m0, s48, 81920\n"
    "s_add_u32 s60, s57, 640\n"
    "buffer_load_dwordx4 v153, s[40:43], s60 offen lds\n"
    "v_mfma_f32_16x16x32_bf16 v[28:31], v[116:119], a[252:255], v[28:31]\n"
    "s_waitcnt lgkmcnt(5)\n"
    "v_mfma_f32_16x16x32_bf16 v[32:35], v[120:123], a[240:243], v[32:35]\n"
    "v_mfma_f32_16x16x32_bf16 v[36:39], v[120:123], a[244:247], v[36:39]\n"
    "v_mfma_f32_16x16x32_bf16 v[40:43], v[120:123], a[248:251], v[40:43]\n"
    "v_mfma_f32_16x16x32_bf16 v[44:47], v[120:123], a[252:255], v[44:47]\n"
    "s_waitcnt lgkmcnt(4)\n"
    "v_mfma_f32_16x16x32_bf16 v[48:51], v[124:127], a[240:243], v[48:51]\n"
    "s_add_u32 m0, s48, 86016\n"
    "s_add_u32 s60, s57, 37504\n"
    "buffer_load_dwordx4 v153, s[40:43], s60 offen lds\n"
    "v_mfma_f32_16x16x32_bf16 v[52:55], v[124:127], a[244:247], v[52:55]\n"
    "v_mfma_f32_16x16x32_bf16 v[56:59], v[124:127], a[248:251], v[56:59]\n"
    "v_mfma_f32_16x16x32_bf16 v[60:63], v[124:127], a[252:255], v[60:63]\n"
    "s_waitcnt lgkmcnt(3)\n"
    "v_mfma_f32_16x16x32_bf16 v[0:3], v[96:99], v[160:163], v[0:3]\n"
    "v_mfma_f32_16x16x32_bf16 v[4:7], v[96:99], v[164:167], v[4:7]\n"
    "ds_read_b128 v[112:115], v133 offset:32768\n"
    "v_mfma_f32_16x16x32_bf16 v[8:11], v[96:99], v[168:171], v[8:11]\n"
    "ds_read_b128 v[116:119], v133 offset:33280\n"
    "v_mfma_f32_16x16x32_bf16 v[12:15], v[96:99], v[172:175], v[12:15]\n"
    "ds_read_b128 v[120:123], v133 offset:36864\n"
    "s_waitcnt lgkmcnt(5)\n"
    "v_mfma_f32_16x16x32_bf16 v[16:19], v[100:103], v[160:163], v[16:19]\n"
    "ds_read_b128 v[124:127], v133 offset:37376\n"
    "v_mfma_f32_16x16x32_bf16 v[20:23], v[100:103], v[164:167], v[20:23]\n"
    "v_mfma_f32_16x16x32_bf16 v[24:27], v[100:103], v[168:171], v[24:27]\n"
    "s_add_u32 m0, s48, 90112\n"
    "s_add_u32 s60, s57, 74368\n"
    "buffer_load_dwordx4 v153, s[40:43], s60 offen lds\n"
    "v_mfma_f32_16x16x32_bf16 v[28:31], v[100:103], v[172:175], v[28:31]\n"
    "s_waitcnt lgkmcnt(5)\n"
    "v_mfma_f32_16x16x32_bf16 v[32:35], v[104:107], v[160:163], v[32:35]\n"
    "v_mfma_f32_16x16x32_bf16 v[36:39], v[104:107], v[164:167], v[36:39]\n"
    "v_mfma_f32_16x16x32_bf16 v[40:43], v[104:107], v[168:171], v[40:43]\n"
    "v_mfma_f32_16x16x32_bf16 v[44:47], v[104:107], v[172:175], v[44:47]\n"
    "s_waitcnt lgkmcnt(4)\n"
    "v_mfma_f32_16x16x32_bf16 v[48:51], v[108:111], v[160:163], v[48:51]\n"
    "s_add_u32 m0, s48, 94208\n"
    "s_add_u32 s60, s57, 111232\n"
    "buffer_load_dwordx4 v153, s[40:43], s60 offen lds\n"
    "v_mfma_f32_16x16x32_bf16 v[52:55], v[108:111], v[164:167], v[52:55]\n"
    "v_mfma_f32_16x16x32_bf16 v[56:59], v[108:111], v[168:171], v[56:59]\n"
    "v_mfma_f32_16x16x32_bf16 v[60:63], v[108:111], v[172:175], v[60:63]\n"
    "s_waitcnt lgkmcnt(0)\n"
    "s_waitcnt vmcnt(12)\n"
    "s_barrier\n"
    "v_mfma_f32_16x16x32_bf16 v[0:3], v[112:115], v[176:179], v[0:3]\n"
    "v_mfma_f32_16x16x32_bf16 v[4:7], v[112:115], v[180:183], v[4:7]\n"
    "ds_read_b128 v[96:99], v128\n"
    "v_mfma_f32_16x16x32_bf16 v[8:11], v[112:115], v[184:187], v[8:11]\n"
    "ds_read_b128 v[100:103], v128 offset:512\n"
    "v_mfma_f32_16x16x32_bf16 v[12:15], v[112:115], v[188:191], v[12:15]\n"
    "ds_read_b128 v[104:107], v128 offset:4096\n"
    "v_mfma_f32_16x16x32_bf16 v[16:19], v[116:119], v[176:179], v[16:19]\n"
    "ds_read_b128 v[108:111], v128 offset:4608\n"
    "v_mfma_f32_16x16x32_bf16 v[20:23], v[116:119], v[180:183], v[20:23]\n"
    "v_mfma_f32_16x16x32_bf16 v[24:27], v[116:119], v[184:187], v[24:27]\n"
    "s_add_u32 m0, s48, 98304\n"
    "s_add_u32 s60, s57, 768\n"
    "buffer_load_dwordx4 v153, s[40:43], s60 offen lds\n"
    "v_mfma_f32_16x16x32_bf16 v[28:31], v[116:119], v[188:191], v[28:31]\n"
    "v_mfma_f32_16x16x32_bf16 v[32:35], v[120:123], v[176:179], v[32:35]\n"
    "v_mfma_f32_16x16x32_bf16 v[36:39], v[120:123], v[180:183], v[36:39]\n"
    "v_mfma_f32_16x16x32_bf16 v[40:43], v[120:123], v[184:187], v[40:43]\n"
    "v_mfma_f32_16x16x32_bf16 v[44:47], v[120:123], v[188:191], v[44:47]\n"
    "v_mfma_f32_16x16x32_bf16 v[48:51], v[124:127], v[176:179], v[48:51]\n"
    "s_add_u32 m0, s48, 102400\n"
    "s_add_u32 s60, s57, 37632\n"
    "buffer_load_dwordx4 v153, s[40:43], s60 offen lds\n"
    "v_mfma_f32_16x16x32_bf16 v[52:55], v[124:127], v[180:183], v[52:55]\n"
    "v_mfma_f32_16x16x32_bf16 v[56:59], v[124:127], v[184:187], v[56:59]\n"
    "v_mfma_f32_16x16x32_bf16 v[60:63], v[124:127], v[188:191], v[60:63]\n"
    "s_nop 3\n"
    "v_cvt_pk_bf16_f32 v64, v0, v1\n"
    "v_cvt_pk_bf16_f32 v65, v2, v3\n"
    "v_cvt_pk_bf16_f32 v66, v16, v17\n"
    "v_cvt_pk_bf16_f32 v67, v18, v19\n"
    "v_cvt_pk_bf16_f32 v68, v4, v5\n"
    "v_cvt_pk_bf16_f32 v69, v6, v7\n"
    "v_cvt_pk_bf16_f32 v70, v20, v21\n"
    "v_cvt_pk_bf16_f32 v71, v22, v23\n"
    "v_cvt_pk_bf16_f32 v72, v8, v9\n"
    "v_cvt_pk_bf16_f32 v73, v10, v11\n"
    "v_cvt_pk_bf16_f32 v74, v24, v25\n"
    "v_cvt_pk_bf16_f32 v75, v26, v27\n"
    "v_cvt_pk_bf16_f32 v76, v12, v13\n"
    "v_cvt_pk_bf16_f32 v77, v14, v15\n"
    "v_cvt_pk_bf16_f32 v78, v28, v29\n"
    "v_cvt_pk_bf16_f32 v79, v30, v31\n"
    "v_cvt_pk_bf16_f32 v80, v32, v33\n"
    "v_cvt_pk_bf16_f32 v81, v34, v35\n"
    "v_cvt_pk_bf16_f32 v82, v48, v49\n"
    "v_cvt_pk_bf16_f32 v83, v50, v51\n"
    "v_cvt_pk_bf16_f32 v84, v36, v37\n"
    "v_cvt_pk_bf16_f32 v85, v38, v39\n"
    "v_cvt_pk_bf16_f32 v86, v52, v53\n"
    "v_cvt_pk_bf16_f32 v87, v54, v55\n"
    "v_cvt_pk_bf16_f32 v88, v40, v41\n"
    "v_cvt_pk_bf16_f32 v89, v42, v43\n"
    "v_cvt_pk_bf16_f32 v90, v56, v57\n"
    "v_cvt_pk_bf16_f32 v91, v58, v59\n"
    "v_cvt_pk_bf16_f32 v92, v44, v45\n"
    "v_cvt_pk_bf16_f32 v93, v46, v47\n"
    "v_cvt_pk_bf16_f32 v94, v60, v61\n"
    "v_cvt_pk_bf16_f32 v95, v62, v63\n"
    "s_mov_b32 s58, s49\n"
    "s_mov_b32 s59, s50\n"
    "s_add_u32 s49, s49, 1048576\n"
    "s_add_u32 s50, s50, s53\n"
    "s_add_u32 s52, s52, 147456\n"
    "s_cmp_eq_u32 s56, 0\n"
    "s_cbranch_scc1 G1I\n"
    "s_cmp_eq_u32 s56, 15\n"
    "s_cbranch_scc0 G1N\n"
    "G1I:\n"
    "s_add_u32 s61, s58, 0\n"
    "buffer_store_dwordx4 v[64:67], v152, s[36:39], s61 offen sc1\n"
    "buffer_store_dwordx4 v[68:71], v152, s[36:39], s61 offen offset:1024 sc1\n"
    "buffer_store_dwordx4 v[72:75], v152, s[36:39], s61 offen offset:2048 sc1\n"
    "buffer_store_dwordx4 v[76:79], v152, s[36:39], s61 offen offset:3072 sc1\n"
    "s_add_u32 s61, s58, 262144\n"
    "buffer_store_dwordx4 v[80:83], v152, s[36:39], s61 offen sc1\n"
    "buffer_store_dwordx4 v[84:87], v152, s[36:39], s61 offen offset:1024 sc1\n"
    "buffer_store_dwordx4 v[88:91], v152, s[36:39], s61 offen offset:2048 sc1\n"
    "buffer_store_dwordx4 v[92:95], v152, s[36:39], s61 offen offset:3072 sc1\n"
    "s_waitcnt vmcnt(0)\n"
    "s_barrier\n"
    "s_mov_b64 exec, s[62:63]\n"
    "buffer_store_dword v152, v152, s[36:39], s59 offen sc1\n"
    "s_mov_b64 exec, -1\n"
    "s_mov_b32 s58, 0x70000000\n"
    "s_mov_b32 s59, 0x70000000\n"
    "G1N:\n"
    "s_add_u32 s56, s56, 1\n"
    "s_cmp_lt_u32 s56, 16\n"
    "s_waitcnt lgkmcnt(0)\n"
    "s_cbranch_scc1 GT1\n"
    "GX:\n"
    "s_waitcnt vmcnt(0)\n"
        : "+{v[128:143]}"(c0), "+{s[36:43]}"(rs), "+{s[48:55]}"(t0) : "{s[44:47]}"(rw), "{v152}"(l16), "{v153}"(xvo), "{v154}"(wvo) :
"v0", "v1", "v2", "v3", "v4", "v5", "v6", "v7", "v8", "v9", "v10", "v11", "v12", "v13", "v14", "v15", "v16", "v17", "v18", "v19", "v20", "v21", "v22", "v23", "v24", "v25", "v26", "v27", "v28", "v29", "v30", "v31", "v32", "v33", "v34", "v35", "v36", "v37", "v38", "v39", "v40", "v41", "v42", "v43", "v44", "v45", "v46", "v47", "v48", "v49", "v50", "v51", "v52", "v53", "v54", "v55", "v56", "v57", "v58", "v59", "v60", "v61", "v62", "v63", "v64", "v65", "v66", "v67", "v68", "v69", "v70", "v71", "v72", "v73", "v74", "v75", "v76", "v77", "v78", "v79", "v80", "v81", "v82", "v83", "v84", "v85", "v86", "v87", "v88", "v89", "v90", "v91", "v92", "v93", "v94", "v95", "v96", "v97", "v98", "v99", "v100", "v101", "v102", "v103", "v104", "v105", "v106", "v107", "v108", "v109", "v110", "v111", "v112", "v113", "v114", "v115", "v116", "v117", "v118", "v119", "v120", "v121", "v122", "v123", "v124", "v125", "v126", "v127", "v144", "v145", "v146", "v147", "v148", "v149", "v150", "v151", "v160", "v161", "v162", "v163", "v164", "v165", "v166", "v167", "v168", "v169", "v170", "v171", "v172", "v173", "v174", "v175", "v176", "v177", "v178", "v179", "v180", "v181", "v182", "v183", "v184", "v185", "v186", "v187", "v188", "v189", "v190", "v191", "a0", "a1", "a2", "a3", "a4", "a5", "a6", "a7", "a8", "a9", "a10", "a11", "a12", "a13", "a14", "a15", "a16", "a17", "a18", "a19", "a20", "a21", "a22", "a23", "a24", "a25", "a26", "a27", "a28", "a29", "a30", "a31", "a32", "a33", "a34", "a35", "a36", "a37", "a38", "a39", "a40", "a41", "a42", "a43", "a44", "a45", "a46", "a47", "a48", "a49", "a50", "a51", "a52", "a53", "a54", "a55", "a56", "a57", "a58", "a59", "a60", "a61", "a62", "a63", "a64", "a65", "a66", "a67", "a68", "a69", "a70", "a71", "a72", "a73", "a74", "a75", "a76", "a77", "a78", "a79", "a80", "a81", "a82", "a83", "a84", "a85", "a86", "a87", "a88", "a89", "a90", "a91", "a92", "a93", "a94", "a95", "a96", "a97", "a98", "a99", "a100", "a101", "a102", "a103", "a104", "a105", "a106", "a107", "a108", "a109", "a110", "a111", "a112", "a113", "a114", "a115", "a116", "a117", "a118", "a119", "a120", "a121", "a122", "a123", "a124", "a125", "a126", "a127", "a128", "a129", "a130", "a131", "a132", "a133", "a134", "a135", "a136", "a137", "a138", "a139", "a140", "a141", "a142", "a143", "a144", "a145", "a146", "a147", "a148", "a149", "a150", "a151", "a152", "a153", "a154", "a155", "a156", "a157", "a158", "a159", "a160", "a161", "a162", "a163", "a164", "a165", "a166", "a167", "a168", "a169", "a170", "a171", "a172", "a173", "a174", "a175", "a176", "a177", "a178", "a179", "a180", "a181", "a182", "a183", "a184", "a185", "a186", "a187", "a188", "a189", "a190", "a191", "a192", "a193", "a194", "a195", "a196", "a197", "a198", "a199", "a200", "a201", "a202", "a203", "a204", "a205", "a206", "a207", "a208", "a209", "a210", "a211", "a212", "a213", "a214", "a215", "a216", "a217", "a218", "a219", "a220", "a221", "a222", "a223", "a224", "a225", "a226", "a227", "a228", "a229", "a230", "a231", "a232", "a233", "a234", "a235", "a236", "a237", "a238", "a239", "a240", "a241", "a242", "a243", "a244", "a245", "a246", "a247", "a248", "a249", "a250", "a251", "a252", "a253", "a254", "a255", "s56", "s57", "s58", "s59", "s60", "s61", "s62", "s63", "vcc", "scc", "memory"
    );
}

DI void l1_role(const Params& p, const int li) {
    const u32 lane = threadIdx.x & 63, w = rfl(threadIdx.x >> 6);
    const u32 g = lane >> 4, n = lane & 15, h = li * 2 + (w >> 1), a = w & 1;
    const rsrc_t rws = mkrs(p.wsa(), WS_END);
    const int qt = 8 * (n >> 2) + 4 * a + (n & 3) - 8 * g;
    const float cs = 0.08838834764831845f * 1.4426950408889634f;
    const v4u ones4 = {0x3F803F80u, 0x3F803F80u, 0x3F803F80u, 0x3F803F80u};
    const v4u ones2 = {0x3F80u, 0x3F80u, 0u, 0u};
    const f4 zero = {0, 0, 0, 0};
    auto tiles = [&](int s) { return wait_ge(rws, lane < 2 ? 16384 + (s >> 2) * 1024 + h * 32 + lane * 16 : 10240 + (s >> 2) * 128 + h * 4, WS_FLAG, 0); };
    v4u q[4], k[8], v[8];
    auto ldq = [&](u32 so) {
#pragma unroll
        for (int i = 0; i < 4; ++i) q[i] = bld4(rws, lane * 16 + a * 1024, WS_Q + so + i * 2048, 1); };
    auto ldk = [&](u32 so) {
#pragma unroll
        for (int i = 0; i < 8; ++i) k[i] = bld4(rws, lane * 16 + (i & 1) * 1024, WS_K + so + (i >> 1) * 2048, 1); };
    auto ldv = [&](u32 so) {
#pragma unroll
        for (int i = 0; i < 8; ++i) v[i] = bld4(rws, lane * 16 + (i & 1) * 1024, WS_VT + so + (i >> 1) * 2048, 1); };
    if (!tiles(0)) return;
    ldq(h * 8192); ldk(h * 8192); ldv(h * 8192);
    int prog = -1;
    for (int s = 0; s < 64; ++s) {
        const u32 sn = min(s + 1, 63) * 262144 + h * 8192;
        f4 s0 = zero, s1 = zero, sm = zero;
#pragma unroll
        for (int i = 0; i < 4; ++i) { s0 = mf32(k[2 * i], q[i], s0); s1 = mf32(k[2 * i + 1], q[i], s1); }
        SB();
        if ((s & 3) == 3 && s < 63 && !tiles(s + 1)) return;
        ldk(sn);
        for (u32 spins = 0; prog <= s; ++spins) {
            CLOB();
            const int f = (int)bld1(rws, (lane & 1) * 128, F_PROG + h * 256);
            prog = min((int)__builtin_amdgcn_readlane(f, 0), (int)__builtin_amdgcn_readlane(f, 1));
            if (prog > s) break;
            __builtin_amdgcn_s_sleep(1);
            if (spins >= SPIN_CAP) { bst1(2u, rws, 0, F_ERR); return; }
        }
        CLOB();
        v4u km[4], vm[4];
#pragma unroll
        for (int i = 0; i < 4; ++i) km[i] = bld4(rws, (g * 8 + (n >> 1)) * 16, WS_KM + s * 65536 + h * 2048 + i * 512, 1);
#pragma unroll
        for (int i = 0; i < 4; ++i) vm[i] = bld4(rws, lane * 16, WS_VM + s * 131072 + h * 4096 + i * 1024, 1);
        SB();
#pragma unroll
        for (int i = 0; i < 4; ++i) sm = mf32(km[i], q[i], sm);
        SB();
        ldq(sn);
        float y[10];
#pragma unroll
        for (int j = 0; j < 4; ++j) { y[j] = j <= qt ? s0[j] * cs : -__builtin_inff(); y[4 + j] = 4 + j <= qt ? s1[j] * cs : -__builtin_inff(); }
        y[8] = sm[0] * cs; y[9] = sm[2] * cs;
        float m = y[0];
#pragma unroll
        for (int j = 1; j < 10; ++j) m = fmaxf(m, y[j]);
        { u32 x = BC(u32, m), z = x; swap16(x, z); m = fmaxf(BC(float, x), BC(float, z)); }
        { float x = m, z = m; swap32(x, z); m = fmaxf(x, z); }
#pragma unroll
        for (int j = 0; j < 10; ++j) y[j] = __builtin_amdgcn_exp2f(y[j] - m);
        const v4u px = {pk(y[0], y[1]), pk(y[2], y[3]), pk(y[4], y[5]), pk(y[6], y[7])};
        const v4u pm = {pk(y[8], y[8]), pk(y[9], y[9]), 0u, 0u};
        f4 l = mf32(ones4, px, zero); l = mf32(ones2, pm, l);
        const float il = __builtin_amdgcn_rcpf(l[0]);
#pragma unroll
        for (int i = 0; i < 4; ++i) {
            f4 o0 = mf32(v[2 * i], px, zero), o1 = mf32(v[2 * i + 1], px, zero);
            o0 = mf32((v4u){vm[i].x, vm[i].y, 0u, 0u}, pm, o0); o1 = mf32((v4u){vm[i].z, vm[i].w, 0u, 0u}, pm, o1);
            o0 *= il; o1 *= il;
            bst4((v4u){pk(o0[0], o0[1]), pk(o0[2], o0[3]), pk(o1[0], o1[1]), pk(o1[2], o1[3])}, rws, lane * 16 + i * 1024, WS_OX + s * 262144 + a * 131072 + h * 4096);
        }
        SB(); CLOB();
        ldv(sn);
        SB();
        waitvm<8>();
        if (lane == 0) __hip_atomic_fetch_add((u32*)(p.wsa() + F_OX + (s * 2 + a) * 64), 1u, __ATOMIC_RELAXED, __HIP_MEMORY_SCOPE_AGENT);
    }
}

DI void l2_role(const Params& p, const int li) {
    const u32 lane = threadIdx.x & 63, w = rfl(threadIdx.x >> 6);
    const u32 g = lane >> 4, n = lane & 15, q4 = n >> 2, p4 = lane & 3, a = li & 1, par = (li >> 1) & 1, cb = li >> 2;
    const rsrc_t rws = mkrs(p.wsa(), WS_END), ro = mkrs(p.Wo, 4096 * 1152);
    v4u wf[32][3];
    {
        u32 vo[3];
#pragma unroll
        for (int i = 0; i < 3; ++i) { const u32 P = i * 64 + lane, r = P / 6; vo[i] = r * 1152 + (P - r * 6) * 16; }
        const u32 lb = w * 36864;
        auto fill = [&](int c) {
#pragma unroll
            for (int i = 0; i < 12; ++i) dma16(ro, vo[i % 3], (w * 1024 + c * 128 + (i / 3) * 32) * 1152 + cb * 96, lb + (c % 3) * 12288 + i * 1024);
        };
        fill(0); fill(1); fill(2);
        const u32 ra = lb + (4 * g + q4) * 96 + p4 * 8;
#pragma unroll
        for (int c = 0; c < 8; ++c) {
            if (c < 6) waitvm<24>(); else if (c == 6) waitvm<12>(); else waitvm<0>();
#pragma unroll
            for (int ks = 0; ks < 4; ++ks)
#pragma unroll
                for (int t = 0; t < 3; ++t) { const v2u x = trrd(ra + (c % 3) * 12288 + ks * 3072 + t * 32), y = trrd(ra + (c % 3) * 12288 + ks * 3072 + 1536 + t * 32); wf[c * 4 + ks][t] = (v4u){x.x, x.y, y.x, y.y}; }
            asm volatile("s_waitcnt lgkmcnt(0)" ::: "memory");
            if (c < 5) fill(c + 3);
        }
    }
    BAR();
    const u32 tt = threadIdx.x & 15, pr = threadIdx.x >> 4;
    u16* const orow = p.out + (size_t)(8 * (tt >> 2) + 4 * a + (tt & 3)) * 1152 + cb * 48 + pr * 4;
    const u32 rr = (pr >> 2) * 1024 + ((pr & 3) * 16 + tt) * 16;
    for (int s = par; s < 64; s += 2) {
        bool ok = true;
        if (w == 0) ok = wait_ge(rws, 0, F_OX + (s * 2 + a) * 64, 31);
        BAR();
        if (!ok) return;
        f4 c0 = {0, 0, 0, 0}, c1 = c0, c2 = c0;
        const u32 so = WS_OX + s * 262144 + a * 131072 + w * 32768;
#pragma unroll
        for (int hf = 0; hf < 2; ++hf) {
            v4u o[16];
#pragma unroll
            for (int i = 0; i < 16; ++i) o[i] = bld4(rws, lane * 16 + (i & 3) * 1024, so + hf * 16384 + (i >> 2) * 4096, 1);
            SB();
#pragma unroll
            for (int i = 0; i < 16; ++i) { c0 = mf32(wf[hf * 16 + i][0], o[i], c0); c1 = mf32(wf[hf * 16 + i][1], o[i], c1); c2 = mf32(wf[hf * 16 + i][2], o[i], c2); }
        }
        const u32 lb = (s & 2) * 8192;
        lwr4(lb + w * 3072 + lane * 16, BC(v4u, c0)); lwr4(lb + w * 3072 + 1024 + lane * 16, BC(v4u, c1)); lwr4(lb + w * 3072 + 2048 + lane * 16, BC(v4u, c2));
        BAR();
        if (pr < 12) {
            const f4 r = (BC(f4, lrd4(lb + rr)) + BC(f4, lrd4(lb + 3072 + rr))) + (BC(f4, lrd4(lb + 6144 + rr)) + BC(f4, lrd4(lb + 9216 + rr)));
            *(v2u*)(orow + (size_t)s * 32 * 1152) = (v2u){pk(r[0], r[1]), pk(r[2], r[3])};
        }
    }
}

DI void kvb_role(const Params& p, const int kt) {
    const u32 lane = threadIdx.x & 63, w = rfl(threadIdx.x >> 6);
    const u32 g = lane >> 4, n = lane & 15;
    const rsrc_t rws = mkrs(p.wsa(), WS_END), rx = mkrs(p.xb, 2048 * 1152), rk = mkrs(p.wk, 576 * 36), rv = mkrs(p.wv, 576 * 36);
    v4u x0[5], x1[5];
#pragma unroll
    for (int i = 0; i < 5; ++i) { const u32 ks = min(w + 4 * i, 17u); x0[i] = bld4(rx, (kt * 32 + n) * 1152 + g * 16, ks * 64, 0); x1[i] = bld4(rx, (kt * 32 + 16 + n) * 1152 + g * 16, ks * 64, 0); }
    {
        u32 a[21], b[21];
#pragma unroll
        for (int i = 0; i < 21; ++i) { a[i] = __builtin_amdgcn_raw_buffer_load_b32(rk, threadIdx.x * 4, i * 1024, 0); b[i] = __builtin_amdgcn_raw_buffer_load_b32(rv, threadIdx.x * 4, i * 1024, 0); }
        SB();
#pragma unroll
        for (int i = 0; i < 21; ++i) { const u32 e = threadIdx.x + i * 256, r = e / 9, c = e - r * 9; *(LAS u32*)(r * 80 + c * 4) = a[i]; *(LAS u32*)(r * 80 + 40 + c * 4) = b[i]; }
    }
    BAR();
    f4 acc[2][3];
#pragma unroll
    for (int m = 0; m < 2; ++m)
#pragma unroll
        for (int t = 0; t < 3; ++t) acc[m][t] = (f4){0, 0, 0, 0};
#pragma unroll
    for (int i = 0; i < 5; ++i) {
        if (w + 4 * i < 18) {
#pragma unroll
            for (int t = 0; t < 3; ++t) {
                const u32 ad = ((w + 4 * i) * 32 + 8 * g + (n >> 2)) * 80 + t * 32 + (lane & 3) * 8;
                const v2u a = trrd(ad), b = trrd(ad + 320);
                const v4u wf = {a.x, a.y, b.x, b.y};
                acc[0][t] = mf32(wf, x0[i], acc[0][t]); acc[1][t] = mf32(wf, x1[i], acc[1][t]);
            }
        }
    }
    const u32 pb = 49152, tb = pb + 4 * 6144;
#pragma unroll
    for (int m = 0; m < 2; ++m)
#pragma unroll
        for (int t = 0; t < 3; ++t) lwr4(pb + w * 6144 + (m * 16 + n) * 192 + (t * 16 + g * 4) * 4, BC(v4u, acc[m][t]));
    BAR();
#pragma unroll
    for (int i = 0; i < 3; ++i) { const u32 e = pb + (threadIdx.x + i * 256) * 8;
        const f2 s = (BC(f2, lrd2(e)) + BC(f2, lrd2(e + 6144))) + (BC(f2, lrd2(e + 12288)) + BC(f2, lrd2(e + 18432)));
        *(LAS u32*)(tb + (threadIdx.x + i * 256) * 4) = pk(s[0], s[1]); }
    BAR();
    const u32 r = lane & 31, hh = lane >> 5, so = WS_BKV + kt * 4096 + w * 1024;
    const u32 k1 = *(LAS u32*)(tb + r * 96 + 32);
    v4u o; float nn = 0.f;
    if (w == 0) {
        o = lrd4(tb + r * 96 + hh * 16);
        nn = hh ? 0.f : bflo(k1) * bflo(k1) + bfhi(k1) * bfhi(k1);
#pragma unroll
        for (int j = 0; j < 4; ++j) nn += bflo(o[j]) * bflo(o[j]) + bfhi(o[j]) * bfhi(o[j]);
        nn += __shfl_xor(nn, 32);
#pragma unroll
        for (int d = 1; d < 32; d <<= 1) nn = fmaxf(nn, __shfl_xor(nn, d));
    } else if (w == 1) o = (v4u){hh ? 0u : k1, 0u, 0u, 0u};
    else {
#pragma unroll
        for (int j = 0; j < 4; ++j) { const u32 ad = tb + (16 * (w - 2) + 8 * (j >> 1) + 4 * hh + 2 * (j & 1)) * 96 + (20 + min(r, 17u)) * 2;
            o[j] = r < 18 ? (u32) * (LAS u16*)ad | ((u32) * (LAS u16*)(ad + 96) << 16) : (r == 18 || r == 22) ? 0x3F803F80u : 0u; }
    }
    bst4(o, rws, lane * 16, so);
    waitvm<0>(); BAR();
    if (threadIdx.x == 0) bst1(BC(u32, nn) & 0x7FFFFFFFu, rws, 0, F_BK + kt * 16);
}
template <class F> DI void b_proj(const u16* W, F af, f4 (&acc)[9]) {
    const u32 lane = threadIdx.x & 63, w = rfl(threadIdx.x >> 6);
    const u32 g = lane >> 4, q4 = (lane & 15) >> 2, p4 = lane & 3;
    const rsrc_t rw = mkrs(W, 576 * 1152);
    u32 vo[9], ra[9];
#pragma unroll
    for (int i = 0; i < 9; ++i) { const u32 P = (w * 9 + i) * 64 + lane, r = P / 72, c = P - r * 72; vo[i] = r * 1152 + ((c ^ ((r & 2) | ((r >> 1) & 4))) << 4); }
#pragma unroll
    for (int t = 0; t < 9; ++t) ra[t] = (8 * g + q4) * 1152 + (((18 * w + 2 * t + (p4 >> 1)) ^ ((q4 & 2) | ((g & 1) << 2))) << 4) + (p4 & 1) * 8;
    auto fill = [&](int ks) {
#pragma unroll
        for (int i = 0; i < 9; ++i) dma16(rw, vo[i], rfl(ks * 36864), rfl((ks % 3) * 36864 + (w * 9 + i) * 1024));
    };
    BAR();
    fill(0); fill(1);
#pragma unroll
    for (int t = 0; t < 9; ++t) acc[t] = (f4){0, 0, 0, 0};
#pragma unroll 1
    for (int k3 = 0; k3 < 6; ++k3)
#pragma unroll
        for (int kk = 0; kk < 3; ++kk) {
            const int ks = k3 * 3 + kk;
            if (ks == 17) waitvm<0>(); else waitvm<9>();
            BAR();
            const v4u x = af(ks);
            v4u wf[9];
#pragma unroll
            for (int t = 0; t < 9; ++t) { const v2u a = trrd(ra[t] + kk * 36864), b = trrd(ra[t] + kk * 36864 + 4608); wf[t] = (v4u){a.x, a.y, b.x, b.y}; }
            if (ks < 16) fill(ks + 2);
#pragma unroll
            for (int t = 0; t < 9; ++t) acc[t] = mf32(wf[t], x, acc[t]);
        }
}
constexpr u32 LB_A = 110592;
DI void b_role(const Params& p, const int bi) {
    const u32 lane = threadIdx.x & 63, w = rfl(threadIdx.x >> 6);
    const u32 g = lane >> 4, n = lane & 15;
    const rsrc_t rws = mkrs(p.wsa(), WS_END);
    for (u32 i = threadIdx.x; i < 16 * 72; i += 256) { const u32 r = i / 72, c = i - r * 72; lwr4(LB_A + r * 1168 + c * 16, *(const v4u*)(p.xb + (size_t)(bi * 16 + r) * 576 + c * 8)); }
    waitvm<0>();
    auto af = [&](int ks) { return lrd4(LB_A + n * 1168 + g * 16 + ks * 64); };
    f4 acc[9];
    b_proj(p.wq, af, acc);
    BAR();
#pragma unroll
    for (int t = 0; t < 9; ++t) lwr2(LB_A + n * 1168 + (144 * w + 16 * t + 4 * g) * 2, (v2u){pk(acc[t][0], acc[t][1]), pk(acc[t][2], acc[t][3])});
    BAR();
    const u32 hd = lane & 31, hh = lane >> 5;
    v4u q0[4]; u32 q1[4]; float mc[4];
    if (!wait_ge(rws, lane * 16, F_BK, 0)) return;
    float kmax = BC(float, bld1(rws, lane * 16, F_BK));
#pragma unroll
    for (int d = 1; d < 64; d <<= 1) kmax = fmaxf(kmax, __shfl_xor(kmax, d));
    const float cs = 0.23570226039551584f * 1.4426950408889634f;
#pragma unroll
    for (int t = 0; t < 4; ++t) {
        const u32 ad = LB_A + (4 * w + t) * 1168 + hd * 36;
#pragma unroll
        for (int j = 0; j < 4; ++j) q0[t][j] = *(LAS u32*)(ad + hh * 16 + j * 4);
        q1[t] = hh ? 0u : *(LAS u32*)(ad + 32);
        float nn = bflo(q1[t]) * bflo(q1[t]) + bfhi(q1[t]) * bfhi(q1[t]);
#pragma unroll
        for (int j = 0; j < 4; ++j) nn += bflo(q0[t][j]) * bflo(q0[t][j]) + bfhi(q0[t][j]) * bfhi(q0[t][j]);
        nn += __shfl_xor(nn, 32);
        mc[t] = -cs * 1.01f * __builtin_sqrtf(nn * kmax);
    }
    f16v o[4];
    const f16v z16 = {0, 0, 0, 0, 0, 0, 0, 0, 0, 0, 0, 0, 0, 0, 0, 0};
    for (int pass = 0;; ++pass) {
        {
            const v16u qa = {q0[0][0], q0[0][1], q0[0][2], q0[0][3], q0[1][0], q0[1][1], q0[1][2], q0[1][3], q0[2][0], q0[2][1], q0[2][2], q0[2][3], q0[3][0], q0[3][1], q0[3][2], q0[3][3]};
            const v16u qb = {q1[0], 0, 0, 0, q1[1], 0, 0, 0, q1[2], 0, 0, 0, q1[3], 0, 0, 0};
            const f4 m4 = {mc[0], mc[1], mc[2], mc[3]};
            const uint64_t a0 = (uint64_t)p.wsa();
            const v4u rs = {rfl((u32)a0), rfl((u32)(a0 >> 32)), WS_END, 0x20000u};
            u32 so = w * 65536;
            asm volatile(
    "s_nop 4\n"
    "s_and_b32 s44, s44, 0x3ffff\n"
    "s_add_u32 s47, s44, s48\n"
    "buffer_load_dwordx4 v[88:91], v36, s[40:43], s47 offen sc1\n"
    "buffer_load_dwordx4 v[92:95], v36, s[40:43], s47 offen offset:1024 sc1\n"
    "buffer_load_dwordx4 v[96:99], v36, s[40:43], s47 offen offset:2048 sc1\n"
    "buffer_load_dwordx4 v[100:103], v36, s[40:43], s47 offen offset:3072 sc1\n"
    "s_add_u32 s44, s44, 4096\n"
    "v_mov_b64 v[128:129], 0\n"
    "v_mov_b64 v[130:131], 0\n"
    "v_mov_b64 v[132:133], 0\n"
    "v_mov_b64 v[134:135], 0\n"
    "v_mov_b64 v[136:137], 0\n"
    "v_mov_b64 v[138:139], 0\n"
    "v_mov_b64 v[140:141], 0\n"
    "v_mov_b64 v[142:143], 0\n"
    "v_mov_b64 v[144:145], 0\n"
    "v_mov_b64 v[146:147], 0\n"
    "v_mov_b64 v[148:149], 0\n"
    "v_mov_b64 v[150:151], 0\n"
    "v_mov_b64 v[152:153], 0\n"
    "v_mov_b64 v[154:155], 0\n"
    "v_mov_b64 v[156:157], 0\n"
    "v_mov_b64 v[158:159], 0\n"
    "v_mov_b64 v[160:161], 0\n"
    "v_mov_b64 v[162:163], 0\n"
    "v_mov_b64 v[164:165], 0\n"
    "v_mov_b64 v[166:167], 0\n"
    "v_mov_b64 v[168:169], 0\n"
    "v_mov_b64 v[170:171], 0\n"
    "v_mov_b64 v[172:173], 0\n"
    "v_mov_b64 v[174:175], 0\n"
    "v_mov_b64 v[176:177], 0\n"
    "v_mov_b64 v[178:179], 0\n"
    "v_mov_b64 v[180:181], 0\n"
    "v_mov_b64 v[182:183], 0\n"
    "v_mov_b64 v[184:185], 0\n"
    "v_mov_b64 v[186:187], 0\n"
    "v_mov_b64 v[188:189], 0\n"
    "v_mov_b64 v[190:191], 0\n"
    "v_mov_b64 v[76:77], 0\n"
    "v_mov_b64 v[78:79], 0\n"
    "s_mov_b32 s45, 31\n"
    "s_mov_b32 s46, 0x3eae1a92\n"
    "s_waitcnt vmcnt(0)\n"
    "v_mov_b64 v[116:117], 0\n"
    "v_mov_b64 v[118:119], 0\n"
    "v_mfma_f32_32x32x16_bf16 v[40:55], v[88:91], v[0:3], 0\n"
    "v_mfma_f32_32x32x16_bf16 v[40:55], v[92:95], v[16:19], v[40:55]\n"
    "s_nop 15\n"
    "s_nop 3\n"
    "BL:\n"
    "v_mfma_f32_32x32x16_bf16 v[176:191], v[116:119], v[76:79], v[176:191]\n"
    "s_and_b32 s44, s44, 0x3ffff\n"
    "s_add_u32 s47, s44, s48\n"
    "buffer_load_dwordx4 v[104:107], v36, s[40:43], s47 offen sc1\n"
    "buffer_load_dwordx4 v[108:111], v36, s[40:43], s47 offen offset:1024 sc1\n"
    "buffer_load_dwordx4 v[112:115], v36, s[40:43], s47 offen offset:2048 sc1\n"
    "buffer_load_dwordx4 v[116:119], v36, s[40:43], s47 offen offset:3072 sc1\n"
    "s_add_u32 s44, s44, 4096\n"
    "v_cvt_pk_bf16_f32 v40, 0, v40\n"
    "v_cvt_pk_bf16_f32 v41, 0, v41\n"
    "v_cvt_pk_bf16_f32 v42, 0, v42\n"
    "v_cvt_pk_bf16_f32 v43, 0, v43\n"
    "v_fma_f32 v40, v40, s46, v32\n"
    "v_fma_f32 v41, v41, s46, v32\n"
    "v_fma_f32 v42, v42, s46, v32\n"
    "v_fma_f32 v43, v43, s46, v32\n"
    "v_mfma_f32_32x32x16_bf16 v[56:71], v[88:91], v[4:7], 0\n"
    "v_exp_f32 v40, v40\n"
    "v_cvt_pk_bf16_f32 v44, 0, v44\n"
    "v_exp_f32 v41, v41\n"
    "v_cvt_pk_bf16_f32 v45, 0, v45\n"
    "v_exp_f32 v42, v42\n"
    "v_cvt_pk_bf16_f32 v46, 0, v46\n"
    "v_exp_f32 v43, v43\n"
    "v_cvt_pk_bf16_f32 v47, 0, v47\n"
    "v_fma_f32 v44, v44, s46, v32\n"
    "v_fma_f32 v45, v45, s46, v32\n"
    "v_fma_f32 v46, v46, s46, v32\n"
    "v_fma_f32 v47, v47, s46, v32\n"
    "v_mfma_f32_32x32x16_bf16 v[56:71], v[92:95], v[20:23], v[56:71]\n"
    "v_exp_f32 v44, v44\n"
    "v_cvt_pk_bf16_f32 v48, 0, v48\n"
    "v_exp_f32 v45, v45\n"
    "v_cvt_pk_bf16_f32 v49, 0, v49\n"
    "v_exp_f32 v46, v46\n"
    "v_cvt_pk_bf16_f32 v50, 0, v50\n"
    "v_exp_f32 v47, v47\n"
    "v_cvt_pk_bf16_f32 v51, 0, v51\n"
    "v_cvt_pk_bf16_f32 v72, v40, v41\n"
    "v_cvt_pk_bf16_f32 v73, v42, v43\n"
    "v_fma_f32 v48, v48, s46, v32\n"
    "v_fma_f32 v49, v49, s46, v32\n"
    "v_fma_f32 v50, v50, s46, v32\n"
    "v_fma_f32 v51, v51, s46, v32\n"
    "v_cvt_pk_bf16_f32 v74, v44, v45\n"
    "v_cvt_pk_bf16_f32 v75, v46, v47\n"
    "v_exp_f32 v48, v48\n"
    "v_cvt_pk_bf16_f32 v52, 0, v52\n"
    "v_exp_f32 v49, v49\n"
    "v_cvt_pk_bf16_f32 v53, 0, v53\n"
    "v_exp_f32 v50, v50\n"
    "v_cvt_pk_bf16_f32 v54, 0, v54\n"
    "v_exp_f32 v51, v51\n"
    "v_cvt_pk_bf16_f32 v55, 0, v55\n"
    "v_mfma_f32_32x32x16_bf16 v[128:143], v[96:99], v[72:75], v[128:143]\n"
    "v_fma_f32 v52, v52, s46, v32\n"
    "v_fma_f32 v53, v53, s46, v32\n"
    "v_fma_f32 v54, v54, s46, v32\n"
    "v_fma_f32 v55, v55, s46, v32\n"
    "v_exp_f32 v52, v52\n"
    "v_cvt_pk_bf16_f32 v76, v48, v49\n"
    "v_exp_f32 v53, v53\n"
    "v_cvt_pk_bf16_f32 v77, v50, v51\n"
    "v_exp_f32 v54, v54\n"
    "v_exp_f32 v55, v55\n"
    "v_cvt_pk_bf16_f32 v78, v52, v53\n"
    "v_cvt_pk_bf16_f32 v79, v54, v55\n"
    "s_nop 1\n"
    "v_mfma_f32_32x32x16_bf16 v[128:143], v[100:103], v[76:79], v[128:143]\n"
    "v_cvt_pk_bf16_f32 v56, 0, v56\n"
    "v_cvt_pk_bf16_f32 v57, 0, v57\n"
    "v_cvt_pk_bf16_f32 v58, 0, v58\n"
    "v_cvt_pk_bf16_f32 v59, 0, v59\n"
    "v_fma_f32 v56, v56, s46, v33\n"
    "v_fma_f32 v57, v57, s46, v33\n"
    "v_fma_f32 v58, v58, s46, v33\n"
    "v_fma_f32 v59, v59, s46, v33\n"
    "v_mfma_f32_32x32x16_bf16 v[40:55], v[88:91], v[8:11], 0\n"
    "v_exp_f32 v56, v56\n"
    "v_cvt_pk_bf16_f32 v60, 0, v60\n"
    "v_exp_f32 v57, v57\n"
    "v_cvt_pk_bf16_f32 v61, 0, v61\n"
    "v_exp_f32 v58, v58\n"
    "v_cvt_pk_bf16_f32 v62, 0, v62\n"
    "v_exp_f32 v59, v59\n"
    "v_cvt_pk_bf16_f32 v63, 0, v63\n"
    "v_fma_f32 v60, v60, s46, v33\n"
    "v_fma_f32 v61, v61, s46, v33\n"
    "v_fma_f32 v62, v62, s46, v33\n"
    "v_fma_f32 v63, v63, s46, v33\n"
    "v_mfma_f32_32x32x16_bf16 v[40:55], v[92:95], v[24:27], v[40:55]\n"
    "v_exp_f32 v60, v60\n"
    "v_cvt_pk_bf16_f32 v64, 0, v64\n"
    "v_exp_f32 v61, v61\n"
    "v_cvt_pk_bf16_f32 v65, 0, v65\n"
    "v_exp_f32 v62, v62\n"
    "v_cvt_pk_bf16_f32 v66, 0, v66\n"
    "v_exp_f32 v63, v63\n"
    "v_cvt_pk_bf16_f32 v67, 0, v67\n"
    "v_cvt_pk_bf16_f32 v72, v56, v57\n"
    "v_cvt_pk_bf16_f32 v73, v58, v59\n"
    "v_fma_f32 v64, v64, s46, v33\n"
    "v_fma_f32 v65, v65, s46, v33\n"
    "v_fma_f32 v66, v66, s46, v33\n"
    "v_fma_f32 v67, v67, s46, v33\n"
    "v_cvt_pk_bf16_f32 v74, v60, v61\n"
    "v_cvt_pk_bf16_f32 v75, v62, v63\n"
    "v_exp_f32 v64, v64\n"
    "v_cvt_pk_bf16_f32 v68, 0, v68\n"
    "v_exp_f32 v65, v65\n"
    "v_cvt_pk_bf16_f32 v69, 0, v69\n"
    "v_exp_f32 v66, v66\n"
    "v_cvt_pk_bf16_f32 v70, 0, v70\n"
    "v_exp_f32 v67, v67\n"
    "v_cvt_pk_bf16_f32 v71, 0, v71\n"
    "v_mfma_f32_32x32x16_bf16 v[144:159], v[96:99], v[72:75], v[144:159]\n"
    "v_fma_f32 v68, v68, s46, v33\n"
    "v_fma_f32 v69, v69, s46, v33\n"
    "v_fma_f32 v70, v70, s46, v33\n"
    "v_fma_f32 v71, v71, s46, v33\n"
    "v_exp_f32 v68, v68\n"
    "v_cvt_pk_bf16_f32 v76, v64, v65\n"
    "v_exp_f32 v69, v69\n"
    "v_cvt_pk_bf16_f32 v77, v66, v67\n"
    "v_exp_f32 v70, v70\n"
    "v_exp_f32 v71, v71\n"
    "v_cvt_pk_bf16_f32 v78, v68, v69\n"
    "v_cvt_pk_bf16_f32 v79, v70, v71\n"
    "s_nop 1\n"
    "v_mfma_f32_32x32x16_bf16 v[144:159], v[100:103], v[76:79], v[144:159]\n"
    "v_cvt_pk_bf16_f32 v40, 0, v40\n"
    "v_cvt_pk_bf16_f32 v41, 0, v41\n"
    "v_cvt_pk_bf16_f32 v42, 0, v42\n"
    "v_cvt_pk_bf16_f32 v43, 0, v43\n"
    "v_fma_f32 v40, v40, s46, v34\n"
    "v_fma_f32 v41, v41, s46, v34\n"
    "v_fma_f32 v42, v42, s46, v34\n"
    "v_fma_f32 v43, v43, s46, v34\n"
    "v_mfma_f32_32x32x16_bf16 v[56:71], v[88:91], v[12:15], 0\n"
    "v_exp_f32 v40, v40\n"
    "v_cvt_pk_bf16_f32 v44, 0, v44\n"
    "v_exp_f32 v41, v41\n"
    "v_cvt_pk_bf16_f32 v45, 0, v45\n"
    "v_exp_f32 v42, v42\n"
    "v_cvt_pk_bf16_f32 v46, 0, v46\n"
    "v_exp_f32 v43, v43\n"
    "v_cvt_pk_bf16_f32 v47, 0, v47\n"
    "v_fma_f32 v44, v44, s46, v34\n"
    "v_fma_f32 v45, v45, s46, v34\n"
    "v_fma_f32 v46, v46, s46, v34\n"
    "v_fma_f32 v47, v47, s46, v34\n"
    "v_mfma_f32_32x32x16_bf16 v[56:71], v[92:95], v[28:31], v[56:71]\n"
    "v_exp_f32 v44, v44\n"
    "v_cvt_pk_bf16_f32 v48, 0, v48\n"
    "v_exp_f32 v45, v45\n"
    "v_cvt_pk_bf16_f32 v49, 0, v49\n"
    "v_exp_f32 v46, v46\n"
    "v_cvt_pk_bf16_f32 v50, 0, v50\n"
    "v_exp_f32 v47, v47\n"
    "v_cvt_pk_bf16_f32 v51, 0, v51\n"
    "v_cvt_pk_bf16_f32 v72, v40, v41\n"
    "v_cvt_pk_bf16_f32 v73, v42, v43\n"
    "v_fma_f32 v48, v48, s46, v34\n"
    "v_fma_f32 v49, v49, s46, v34\n"
    "v_fma_f32 v50, v50, s46, v34\n"
    "v_fma_f32 v51, v51, s46, v34\n"
    "v_cvt_pk_bf16_f32 v74, v44, v45\n"
    "v_cvt_pk_bf16_f32 v75, v46, v47\n"
    "v_exp_f32 v48, v48\n"
    "v_cvt_pk_bf16_f32 v52, 0, v52\n"
    "v_exp_f32 v49, v49\n"
    "v_cvt_pk_bf16_f32 v53, 0, v53\n"
    "v_exp_f32 v50, v50\n"
    "v_cvt_pk_bf16_f32 v54, 0, v54\n"
    "v_exp_f32 v51, v51\n"
    "v_cvt_pk_bf16_f32 v55, 0, v55\n"
    "v_mfma_f32_32x32x16_bf16 v[160:175], v[96:99], v[72:75], v[160:175]\n"
    "v_fma_f32 v52, v52, s46, v34\n"
    "v_fma_f32 v53, v53, s46, v34\n"
    "v_fma_f32 v54, v54, s46, v34\n"
    "v_fma_f32 v55, v55, s46, v34\n"
    "v_exp_f32 v52, v52\n"
    "v_cvt_pk_bf16_f32 v76, v48, v49\n"
    "v_exp_f32 v53, v53\n"
    "v_cvt_pk_bf16_f32 v77, v50, v51\n"
    "v_exp_f32 v54, v54\n"
    "v_exp_f32 v55, v55\n"
    "v_cvt_pk_bf16_f32 v78, v52, v53\n"
    "v_cvt_pk_bf16_f32 v79, v54, v55\n"
    "s_nop 1\n"
    "v_mfma_f32_32x32x16_bf16 v[160:175], v[100:103], v[76:79], v[160:175]\n"
    "v_cvt_pk_bf16_f32 v56, 0, v56\n"
    "v_cvt_pk_bf16_f32 v57, 0, v57\n"
    "v_cvt_pk_bf16_f32 v58, 0, v58\n"
    "v_cvt_pk_bf16_f32 v59, 0, v59\n"
    "v_fma_f32 v56, v56, s46, v35\n"
    "v_fma_f32 v57, v57, s46, v35\n"
    "v_fma_f32 v58, v58, s46, v35\n"
    "v_fma_f32 v59, v59, s46, v35\n"
    "s_waitcnt vmcnt(0)\n"
    "v_mfma_f32_32x32x16_bf16 v[40:55], v[104:107], v[0:3], 0\n"
    "v_exp_f32 v56, v56\n"
    "v_cvt_pk_bf16_f32 v60, 0, v60\n"
    "v_exp_f32 v57, v57\n"
    "v_cvt_pk_bf16_f32 v61, 0, v61\n"
    "v_exp_f32 v58, v58\n"
    "v_cvt_pk_bf16_f32 v62, 0, v62\n"
    "v_exp_f32 v59, v59\n"
    "v_cvt_pk_bf16_f32 v63, 0, v63\n"
    "v_fma_f32 v60, v60, s46, v35\n"
    "v_fma_f32 v61, v61, s46, v35\n"
    "v_fma_f32 v62, v62, s46, v35\n"
    "v_fma_f32 v63, v63, s46, v35\n"
    "v_mfma_f32_32x32x16_bf16 v[40:55], v[108:111], v[16:19], v[40:55]\n"
    "v_exp_f32 v60, v60\n"
    "v_cvt_pk_bf16_f32 v64, 0, v64\n"
    "v_exp_f32 v61, v61\n"
    "v_cvt_pk_bf16_f32 v65, 0, v65\n"
    "v_exp_f32 v62, v62\n"
    "v_cvt_pk_bf16_f32 v66, 0, v66\n"
    "v_exp_f32 v63, v63\n"
    "v_cvt_pk_bf16_f32 v67, 0, v67\n"
    "v_cvt_pk_bf16_f32 v72, v56, v57\n"
    "v_cvt_pk_bf16_f32 v73, v58, v59\n"
    "v_fma_f32 v64, v64, s46, v35\n"
    "v_fma_f32 v65, v65, s46, v35\n"
    "v_fma_f32 v66, v66, s46, v35\n"
    "v_fma_f32 v67, v67, s46, v35\n"
    "v_cvt_pk_bf16_f32 v74, v60, v61\n"
    "v_cvt_pk_bf16_f32 v75, v62, v63\n"
    "v_exp_f32 v64, v64\n"
    "v_cvt_pk_bf16_f32 v68, 0, v68\n"
    "v_exp_f32 v65, v65\n"
    "v_cvt_pk_bf16_f32 v69, 0, v69\n"
    "v_exp_f32 v66, v66\n"
    "v_cvt_pk_bf16_f32 v70, 0, v70\n"
    "v_exp_f32 v67, v67\n"
    "v_cvt_pk_bf16_f32 v71, 0, v71\n"
    "v_mfma_f32_32x32x16_bf16 v[176:191], v[96:99], v[72:75], v[176:191]\n"
    "v_fma_f32 v68, v68, s46, v35\n"
    "v_fma_f32 v69, v69, s46, v35\n"
    "v_fma_f32 v70, v70, s46, v35\n"
    "v_fma_f32 v71, v71, s46, v35\n"
    "v_exp_f32 v68, v68\n"
    "v_cvt_pk_bf16_f32 v76, v64, v65\n"
    "v_exp_f32 v69, v69\n"
    "v_cvt_pk_bf16_f32 v77, v66, v67\n"
    "v_exp_f32 v70, v70\n"
    "v_exp_f32 v71, v71\n"
    "v_cvt_pk_bf16_f32 v78, v68, v69\n"
    "v_cvt_pk_bf16_f32 v79, v70, v71\n"
    "s_nop 1\n"
    "v_mfma_f32_32x32x16_bf16 v[176:191], v[100:103], v[76:79], v[176:191]\n"
    "s_and_b32 s44, s44, 0x3ffff\n"
    "s_add_u32 s47, s44, s48\n"
    "buffer_load_dwordx4 v[88:91], v36, s[40:43], s47 offen sc1\n"
    "buffer_load_dwordx4 v[92:95], v36, s[40:43], s47 offen offset:1024 sc1\n"
    "buffer_load_dwordx4 v[96:99], v36, s[40:43], s47 offen offset:2048 sc1\n"
    "buffer_load_dwordx4 v[100:103], v36, s[40:43], s47 offen offset:3072 sc1\n"
    "s_add_u32 s44, s44, 4096\n"
    "v_cvt_pk_bf16_f32 v40, 0, v40\n"
    "v_cvt_pk_bf16_f32 v41, 0, v41\n"
    "v_cvt_pk_bf16_f32 v42, 0, v42\n"
    "v_cvt_pk_bf16_f32 v43, 0, v43\n"
    "v_fma_f32 v40, v40, s46, v32\n"
    "v_fma_f32 v41, v41, s46, v32\n"
    "v_fma_f32 v42, v42, s46, v32\n"
    "v_fma_f32 v43, v43, s46, v32\n"
    "v_mfma_f32_32x32x16_bf16 v[56:71], v[104:107], v[4:7], 0\n"
    "v_exp_f32 v40, v40\n"
    "v_cvt_pk_bf16_f32 v44, 0, v44\n"
    "v_exp_f32 v41, v41\n"
    "v_cvt_pk_bf16_f32 v45, 0, v45\n"
    "v_exp_f32 v42, v42\n"
    "v_cvt_pk_bf16_f32 v46, 0, v46\n"
    "v_exp_f32 v43, v43\n"
    "v_cvt_pk_bf16_f32 v47, 0, v47\n"
    "v_fma_f32 v44, v44, s46, v32\n"
    "v_fma_f32 v45, v45, s46, v32\n"
    "v_fma_f32 v46, v46, s46, v32\n"
    "v_fma_f32 v47, v47, s46, v32\n"
    "v_mfma_f32_32x32x16_bf16 v[56:71], v[108:111], v[20:23], v[56:71]\n"
    "v_exp_f32 v44, v44\n"
    "v_cvt_pk_bf16_f32 v48, 0, v48\n"
    "v_exp_f32 v45, v45\n"
    "v_cvt_pk_bf16_f32 v49, 0, v49\n"
    "v_exp_f32 v46, v46\n"
    "v_cvt_pk_bf16_f32 v50, 0, v50\n"
    "v_exp_f32 v47, v47\n"
    "v_cvt_pk_bf16_f32 v51, 0, v51\n"
    "v_cvt_pk_bf16_f32 v72, v40, v41\n"
    "v_cvt_pk_bf16_f32 v73, v42, v43\n"
    "v_fma_f32 v48, v48, s46, v32\n"
    "v_fma_f32 v49, v49, s46, v32\n"
    "v_fma_f32 v50, v50, s46, v32\n"
    "v_fma_f32 v51, v51, s46, v32\n"
    "v_cvt_pk_bf16_f32 v74, v44, v45\n"
    "v_cvt_pk_bf16_f32 v75, v46, v47\n"
    "v_exp_f32 v48, v48\n"
    "v_cvt_pk_bf16_f32 v52, 0, v52\n"
    "v_exp_f32 v49, v49\n"
    "v_cvt_pk_bf16_f32 v53, 0, v53\n"
    "v_exp_f32 v50, v50\n"
    "v_cvt_pk_bf16_f32 v54, 0, v54\n"
    "v_exp_f32 v51, v51\n"
    "v_cvt_pk_bf16_f32 v55, 0, v55\n"
    "v_mfma_f32_32x32x16_bf16 v[128:143], v[112:115], v[72:75], v[128:143]\n"
    "v_fma_f32 v52, v52, s46, v32\n"
    "v_fma_f32 v53, v53, s46, v32\n"
    "v_fma_f32 v54, v54, s46, v32\n"
    "v_fma_f32 v55, v55, s46, v32\n"
    "v_exp_f32 v52, v52\n"
    "v_cvt_pk_bf16_f32 v76, v48, v49\n"
    "v_exp_f32 v53, v53\n"
    "v_cvt_pk_bf16_f32 v77, v50, v51\n"
    "v_exp_f32 v54, v54\n"
    "v_exp_f32 v55, v55\n"
    "v_cvt_pk_bf16_f32 v78, v52, v53\n"
    "v_cvt_pk_bf16_f32 v79, v54, v55\n"
    "s_nop 1\n"
    "v_mfma_f32_32x32x16_bf16 v[128:143], v[116:119], v[76:79], v[128:143]\n"
    "v_cvt_pk_bf16_f32 v56, 0, v56\n"
    "v_cvt_pk_bf16_f32 v57, 0, v57\n"
    "v_cvt_pk_bf16_f32 v58, 0, v58\n"
    "v_cvt_pk_bf16_f32 v59, 0, v59\n"
    "v_fma_f32 v56, v56, s46, v33\n"
    "v_fma_f32 v57, v57, s46, v33\n"
    "v_fma_f32 v58, v58, s46, v33\n"
    "v_fma_f32 v59, v59, s46, v33\n"
    "v_mfma_f32_32x32x16_bf16 v[40:55], v[104:107], v[8:11], 0\n"
    "v_exp_f32 v56, v56\n"
    "v_cvt_pk_bf16_f32 v60, 0, v60\n"
    "v_exp_f32 v57, v57\n"
    "v_cvt_pk_bf16_f32 v61, 0, v61\n"
    "v_exp_f32 v58, v58\n"
    "v_cvt_pk_bf16_f32 v62, 0, v62\n"
    "v_exp_f32 v59, v59\n"
    "v_cvt_pk_bf16_f32 v63, 0, v63\n"
    "v_fma_f32 v60, v60, s46, v33\n"
    "v_fma_f32 v61, v61, s46, v33\n"
    "v_fma_f32 v62, v62, s46, v33\n"
    "v_fma_f32 v63, v63, s46, v33\n"
    "v_mfma_f32_32x32x16_bf16 v[40:55], v[108:111], v[24:27], v[40:55]\n"
    "v_exp_f32 v60, v60\n"
    "v_cvt_pk_bf16_f32 v64, 0, v64\n"
    "v_exp_f32 v61, v61\n"
    "v_cvt_pk_bf16_f32 v65, 0, v65\n"
    "v_exp_f32 v62, v62\n"
    "v_cvt_pk_bf16_f32 v66, 0, v66\n"
    "v_exp_f32 v63, v63\n"
    "v_cvt_pk_bf16_f32 v67, 0, v67\n"
    "v_cvt_pk_bf16_f32 v72, v56, v57\n"
    "v_cvt_pk_bf16_f32 v73, v58, v59\n"
    "v_fma_f32 v64, v64, s46, v33\n"
    "v_fma_f32 v65, v65, s46, v33\n"
    "v_fma_f32 v66, v66, s46, v33\n"
    "v_fma_f32 v67, v67, s46, v33\n"
    "v_cvt_pk_bf16_f32 v74, v60, v61\n"
    "v_cvt_pk_bf16_f32 v75, v62, v63\n"
    "v_exp_f32 v64, v64\n"
    "v_cvt_pk_bf16_f32 v68, 0, v68\n"
    "v_exp_f32 v65, v65\n"
    "v_cvt_pk_bf16_f32 v69, 0, v69\n"
    "v_exp_f32 v66, v66\n"
    "v_cvt_pk_bf16_f32 v70, 0, v70\n"
    "v_exp_f32 v67, v67\n"
    "v_cvt_pk_bf16_f32 v71, 0, v71\n"
    "v_mfma_f32_32x32x16_bf16 v[144:159], v[112:115], v[72:75], v[144:159]\n"
    "v_fma_f32 v68, v68, s46, v33\n"
    "v_fma_f32 v69, v69, s46, v33\n"
    "v_fma_f32 v70, v70, s46, v33\n"
    "v_fma_f32 v71, v71, s46, v33\n"
    "v_exp_f32 v68, v68\n"
    "v_cvt_pk_bf16_f32 v76, v64, v65\n"
    "v_exp_f32 v69, v69\n"
    "v_cvt_pk_bf16_f32 v77, v66, v67\n"
    "v_exp_f32 v70, v70\n"
    "v_exp_f32 v71, v71\n"
    "v_cvt_pk_bf16_f32 v78, v68, v69\n"
    "v_cvt_pk_bf16_f32 v79, v70, v71\n"
    "s_nop 1\n"
    "v_mfma_f32_32x32x16_bf16 v[144:159], v[116:119], v[76:79], v[144:159]\n"
    "v_cvt_pk_bf16_f32 v40, 0, v40\n"
    "v_cvt_pk_bf16_f32 v41, 0, v41\n"
    "v_cvt_pk_bf16_f32 v42, 0, v42\n"
    "v_cvt_pk_bf16_f32 v43, 0, v43\n"
    "v_fma_f32 v40, v40, s46, v34\n"
    "v_fma_f32 v41, v41, s46, v34\n"
    "v_fma_f32 v42, v42, s46, v34\n"
    "v_fma_f32 v43, v43, s46, v34\n"
    "v_mfma_f32_32x32x16_bf16 v[56:71], v[104:107], v[12:15], 0\n"
    "v_exp_f32 v40, v40\n"
    "v_cvt_pk_bf16_f32 v44, 0, v44\n"
    "v_exp_f32 v41, v41\n"
    "v_cvt_pk_bf16_f32 v45, 0, v45\n"
    "v_exp_f32 v42, v42\n"
    "v_cvt_pk_bf16_f32 v46, 0, v46\n"
    "v_exp_f32 v43, v43\n"
    "v_cvt_pk_bf16_f32 v47, 0, v47\n"
    "v_fma_f32 v44, v44, s46, v34\n"
    "v_fma_f32 v45, v45, s46, v34\n"
    "v_fma_f32 v46, v46, s46, v34\n"
    "v_fma_f32 v47, v47, s46, v34\n"
    "v_mfma_f32_32x32x16_bf16 v[56:71], v[108:111], v[28:31], v[56:71]\n"
    "v_exp_f32 v44, v44\n"
    "v_cvt_pk_bf16_f32 v48, 0, v48\n"
    "v_exp_f32 v45, v45\n"
    "v_cvt_pk_bf16_f32 v49, 0, v49\n"
    "v_exp_f32 v46, v46\n"
    "v_cvt_pk_bf16_f32 v50, 0, v50\n"
    "v_exp_f32 v47, v47\n"
    "v_cvt_pk_bf16_f32 v51, 0, v51\n"
    "v_cvt_pk_bf16_f32 v72, v40, v41\n"
    "v_cvt_pk_bf16_f32 v73, v42, v43\n"
    "v_fma_f32 v48, v48, s46, v34\n"
    "v_fma_f32 v49, v49, s46, v34\n"
    "v_fma_f32 v50, v50, s46, v34\n"
    "v_fma_f32 v51, v51, s46, v34\n"
    "v_cvt_pk_bf16_f32 v74, v44, v45\n"
    "v_cvt_pk_bf16_f32 v75, v46, v47\n"
    "v_exp_f32 v48, v48\n"
    "v_cvt_pk_bf16_f32 v52, 0, v52\n"
    "v_exp_f32 v49, v49\n"
    "v_cvt_pk_bf16_f32 v53, 0, v53\n"
    "v_exp_f32 v50, v50\n"
    "v_cvt_pk_bf16_f32 v54, 0, v54\n"
    "v_exp_f32 v51, v51\n"
    "v_cvt_pk_bf16_f32 v55, 0, v55\n"
    "v_mfma_f32_32x32x16_bf16 v[160:175], v[112:115], v[72:75], v[160:175]\n"
    "v_fma_f32 v52, v52, s46, v34\n"
    "v_fma_f32 v53, v53, s46, v34\n"
    "v_fma_f32 v54, v54, s46, v34\n"
    "v_fma_f32 v55, v55, s46, v34\n"
    "v_exp_f32 v52, v52\n"
    "v_cvt_pk_bf16_f32 v76, v48, v49\n"
    "v_exp_f32 v53, v53\n"
    "v_cvt_pk_bf16_f32 v77, v50, v51\n"
    "v_exp_f32 v54, v54\n"
    "v_exp_f32 v55, v55\n"
    "v_cvt_pk_bf16_f32 v78, v52, v53\n"
    "v_cvt_pk_bf16_f32 v79, v54, v55\n"
    "s_nop 1\n"
    "v_mfma_f32_32x32x16_bf16 v[160:175], v[116:119], v[76:79], v[160:175]\n"
    "v_cvt_pk_bf16_f32 v56, 0, v56\n"
    "v_cvt_pk_bf16_f32 v57, 0, v57\n"
    "v_cvt_pk_bf16_f32 v58, 0, v58\n"
    "v_cvt_pk_bf16_f32 v59, 0, v59\n"
    "v_fma_f32 v56, v56, s46, v35\n"
    "v_fma_f32 v57, v57, s46, v35\n"
    "v_fma_f32 v58, v58, s46, v35\n"
    "v_fma_f32 v59, v59, s46, v35\n"
    "s_waitcnt vmcnt(0)\n"
    "v_mfma_f32_32x32x16_bf16 v[40:55], v[88:91], v[0:3], 0\n"
    "v_exp_f32 v56, v56\n"
    "v_cvt_pk_bf16_f32 v60, 0, v60\n"
    "v_exp_f32 v57, v57\n"
    "v_cvt_pk_bf16_f32 v61, 0, v61\n"
    "v_exp_f32 v58, v58\n"
    "v_cvt_pk_bf16_f32 v62, 0, v62\n"
    "v_exp_f32 v59, v59\n"
    "v_cvt_pk_bf16_f32 v63, 0, v63\n"
    "v_fma_f32 v60, v60, s46, v35\n"
    "v_fma_f32 v61, v61, s46, v35\n"
    "v_fma_f32 v62, v62, s46, v35\n"
    "v_fma_f32 v63, v63, s46, v35\n"
    "v_mfma_f32_32x32x16_bf16 v[40:55], v[92:95], v[16:19], v[40:55]\n"
    "v_exp_f32 v60, v60\n"
    "v_cvt_pk_bf16_f32 v64, 0, v64\n"
    "v_exp_f32 v61, v61\n"
    "v_cvt_pk_bf16_f32 v65, 0, v65\n"
    "v_exp_f32 v62, v62\n"
    "v_cvt_pk_bf16_f32 v66, 0, v66\n"
    "v_exp_f32 v63, v63\n"
    "v_cvt_pk_bf16_f32 v67, 0, v67\n"
    "v_cvt_pk_bf16_f32 v72, v56, v57\n"
    "v_cvt_pk_bf16_f32 v73, v58, v59\n"
    "v_fma_f32 v64, v64, s46, v35\n"
    "v_fma_f32 v65, v65, s46, v35\n"
    "v_fma_f32 v66, v66, s46, v35\n"
    "v_fma_f32 v67, v67, s46, v35\n"
    "v_cvt_pk_bf16_f32 v74, v60, v61\n"
    "v_cvt_pk_bf16_f32 v75, v62, v63\n"
    "v_exp_f32 v64, v64\n"
    "v_cvt_pk_bf16_f32 v68, 0, v68\n"
    "v_exp_f32 v65, v65\n"
    "v_cvt_pk_bf16_f32 v69, 0, v69\n"
    "v_exp_f32 v66, v66\n"
    "v_cvt_pk_bf16_f32 v70, 0, v70\n"
    "v_exp_f32 v67, v67\n"
    "v_cvt_pk_bf16_f32 v71, 0, v71\n"
    "v_mfma_f32_32x32x16_bf16 v[176:191], v[112:115], v[72:75], v[176:191]\n"
    "v_fma_f32 v68, v68, s46, v35\n"
    "v_fma_f32 v69, v69, s46, v35\n"
    "v_fma_f32 v70, v70, s46, v35\n"
    "v_fma_f32 v71, v71, s46, v35\n"
    "v_exp_f32 v68, v68\n"
    "v_cvt_pk_bf16_f32 v76, v64, v65\n"
    "v_exp_f32 v69, v69\n"
    "v_cvt_pk_bf16_f32 v77, v66, v67\n"
    "v_exp_f32 v70, v70\n"
    "v_exp_f32 v71, v71\n"
    "v_cvt_pk_bf16_f32 v78, v68, v69\n"
    "v_cvt_pk_bf16_f32 v79, v70, v71\n"
    "s_sub_u32 s45, s45, 1\n"
    "s_cbranch_scc0 BL\n"
    "s_nop 1\n"
    "v_mfma_f32_32x32x16_bf16 v[176:191], v[116:119], v[76:79], v[176:191]\n"
    "s_waitcnt vmcnt(0)\n"
    "s_nop 15\n"
    "s_nop 7\n"
                : "={v[128:143]}"(o[0]), "={v[144:159]}"(o[1]), "={v[160:175]}"(o[2]), "={v[176:191]}"(o[3]), "+{s44}"(so)
                : "{v[0:15]}"(qa), "{v[16:31]}"(qb), "{v[32:35]}"(m4), "{v36}"(lane * 16), "{s[40:43]}"(rs), "{s48}"(WS_BKV) :
"v40", "v41", "v42", "v43", "v44", "v45", "v46", "v47", "v48", "v49", "v50", "v51", "v52", "v53", "v54", "v55", "v56", "v57", "v58", "v59", "v60", "v61", "v62", "v63", "v64", "v65", "v66", "v67", "v68", "v69", "v70", "v71", "v72", "v73", "v74", "v75", "v76", "v77", "v78", "v79", "v80", "v81", "v82", "v83", "v84", "v85", "v86", "v87", "v88", "v89", "v90", "v91", "v92", "v93", "v94", "v95", "v96", "v97", "v98", "v99", "v100", "v101", "v102", "v103", "v104", "v105", "v106", "v107", "v108", "v109", "v110", "v111", "v112", "v113", "v114", "v115", "v116", "v117", "v118", "v119", "s45", "s46", "s47", "vcc", "scc", "memory"
            );
        }
        if (pass || __all(fminf(fminf(o[0][10], o[1][10]), fminf(o[2][10], o[3][10])) >= 0x1p-80f)) break;
        float mx[4] = {-__builtin_inff(), -__builtin_inff(), -__builtin_inff(), -__builtin_inff()};
#pragma unroll 1
        for (int kt = 0; kt < 64; ++kt) {
            const v4u a = bld4(rws, lane * 16, WS_BKV + kt * 4096, 1), b = bld4(rws, lane * 16 + 1024, WS_BKV + kt * 4096, 1);
#pragma unroll
            for (int t = 0; t < 4; ++t) {
                f16v s = mf3(a, q0[t], z16); s = mf3(b, (v4u){q1[t], 0u, 0u, 0u}, s);
#pragma unroll
                for (int j = 0; j < 16; ++j) mx[t] = fmaxf(mx[t], BC(float, pk(0.f, s[j])));
            }
        }
#pragma unroll
        for (int t = 0; t < 4; ++t) mc[t] = -cs * fmaxf(mx[t], __shfl_xor(mx[t], 32));
    }
    BAR();
#pragma unroll
    for (int t = 0; t < 4; ++t) {
        const float il = __builtin_amdgcn_rcpf(o[t][10]);
        const u32 ad = LB_A + (4 * w + t) * 1168 + hd * 36 + hh * 8;
#pragma unroll
        for (int j = 0; j < 2; ++j) { *(LAS u32*)(ad + j * 16) = pk(o[t][4 * j] * il, o[t][4 * j + 1] * il); *(LAS u32*)(ad + j * 16 + 4) = pk(o[t][4 * j + 2] * il, o[t][4 * j + 3] * il); }
        if (!hh) *(LAS u32*)(ad + 32) = pk(o[t][8] * il, o[t][9] * il);
    }
    waitvm<0>();
    b_proj(p.wo, af, acc);
#pragma unroll
    for (int t = 0; t < 9; ++t) *(v2u*)(p.out + (size_t)(bi * 16 + n) * 1152 + 576 + 144 * w + 16 * t + 4 * g) = (v2u){pk(acc[t][0], acc[t][1]), pk(acc[t][2], acc[t][3])};
}
__global__ void __launch_bounds__(256, 1) mk(const Params p) {
    extern __shared__ char lds_[];
    const int b = blockIdx.x, xcd = b & 7, idx = b >> 3;
    int bi = -1;
    if (xcd < 4) {
        if (idx < 16) { x_role_asm(p, (idx >> 1) * 8 + xcd * 2 + (idx & 1), xcd); return; }
        bi = (idx - 16) * 4 + xcd;
        kvb_role(p, bi);
    } else {
        const int xq = xcd - 4;
        if (idx < 24) {
            g_role(p, idx < 8 ? 1 : idx < 16 ? 2 : 0, xq * 8 + (idx & 7));
            if (idx < 12) { l2_role(p, idx * 4 + xq); return; }
            bi = 80 + xq * 12 + idx - 12;
        } else if (idx < 28) { l1_role(p, xq * 4 + idx - 24); return; }
        else bi = 64 + xq * 4 + idx - 28;
    }
    b_role(p, bi);
}
__global__ void __launch_bounds__(256) init(const Params p) {
    const int b = blockIdx.x;
    const rsrc_t rws = mkrs(p.wsa(), WS_END);
    if (b == 0 || b == 8) {
        if (threadIdx.x >= 64) return;
        const int w = b >> 3;
        u32 best = ~0u;
        for (int i = 0; i < 4; ++i) {
            unsigned long long a, e;
            asm volatile("s_memtime %0\n\ts_waitcnt lgkmcnt(0)" : "=s"(a) :: "memory");
            bst1(0xFFFFFFFFu, rws, 0, WS_INIT_BYTES + w * 8192 + (i & 1) * 4096 + (i >> 1) * 16384);
            asm volatile("s_waitcnt vmcnt(0)\n\ts_memtime %0\n\ts_waitcnt lgkmcnt(0)" : "=s"(e) :: "memory");
            best = min(best, (u32)(e - a));
        }
        if (threadIdx.x == 0) *(u32*)(p.wsa() + F_CSEL + 16 * w) = best;
        return;
    }
    const v4u ff = {~0u, ~0u, ~0u, ~0u};
    for (u32 i = (b < 8 ? b - 1 : b - 2) * 256 + threadIdx.x; i < WS_INIT_BYTES / 16; i += 254 * 256) if ((i >> 1) != F_CSEL / 32) ((v4u*)p.wsa())[i] = ff;
}
constexpr int g_d1 = 2800, g_d2 = 1200, g_d3 = 2, g_d4 = 20, g_d5 = 3, g_mask = 15;
extern "C" void kernel_launch(void* const* d_in, const int* in_sizes, int n_in, void* d_out, int out_size, void* d_ws, size_t ws_size, hipStream_t st) {
    Params p{(const u16*)d_in[0], (const u16*)d_in[1], (const u16*)d_in[2], (const u16*)d_in[3], (const u16*)d_in[4], (const u16*)d_in[5],
             (const u16*)d_in[7], (const u16*)d_in[8], (const u16*)d_in[9], (const u16*)d_in[10], (const u16*)d_in[11], (u16*)d_out,
             (char*)d_ws, g_d1, g_d2, g_d3, g_d4, g_d5, g_mask};
    init<<<256, 256, 0, st>>>(p);
    (void)hipFuncSetAttribute((const void*)mk, hipFuncAttributeMaxDynamicSharedMemorySize, 163840);
    mk<<<256, 256, 163840, st>>>(p);
}
```
